```python
import torch

def _softmax(x, axis=-1):
    return torch.softmax(x, dim=axis)

def _gelu(x):
    return torch.nn.functional.gelu(x, approximate='tanh')

def _silu(x):
    return torch.nn.functional.silu(x)

def _sigmoid(x):
    return torch.sigmoid(x)

def _layernorm(x, axis=-1, eps=1e-05):
    m = x.mean(dim=axis, keepdim=True)
    v = x.var(dim=axis, keepdim=True, unbiased=False)
    return (x - m) / torch.sqrt(v + eps)

def _scatter_add(tgt, idx, src, axis=0):
    return tgt.clone().index_add_(axis, idx, src)

def _segment_sum(src, seg, n):
    out = torch.zeros((n,) + src.shape[1:], dtype=src.dtype)
    return out.index_add_(0, seg, src)

def _pad(x, pw):
    flat = tuple((v for pair in reversed(pw) for v in pair))
    return torch.nn.functional.pad(x, flat)
kernel_name = 'recurrent_scan__spectral_gfnet__mixer_gmlp__prompt'

def setup_inputs():

    _w = lambda a: (lambda t: t.to(torch.bfloat16) if t.is_floating_point() else t)(torch.as_tensor(a))

    def _A_setup():
        B, S, D = (1, 2048, 448)
        Df = D // 2 + 1
        return {'x': _w((lambda *s: torch.randn(*s, dtype=torch.bfloat16) * 0.05)(B, S, D)), 'w_r': _w((lambda *s: torch.randn(*s, dtype=torch.bfloat16) * 0.2)(S, Df) * 0.02), 'w_i': _w((lambda *s: torch.randn(*s, dtype=torch.bfloat16) * 0.2)(S, Df) * 0.02)}
    _A_in = _A_setup()

    def _B_setup():
        B, S, D, F = (1, 2048, 448, 1792)
        Fh = F // 2
        return {'x': _w((lambda *s: torch.randn(*s, dtype=torch.bfloat16) * 0.05)(B, S, D)), 'w_in': _w((lambda *s: torch.randn(*s, dtype=torch.bfloat16) * 0.4)(D, 2 * Fh)), 'w_sp': _w(0.01 * (lambda *s: torch.randn(*s, dtype=torch.bfloat16) * 0.4)(S, S)), 'b_sp': _w(1.0 + 0.01 * (lambda *s: torch.randn(*s, dtype=torch.bfloat16) * 0.1)(S)), 'w_out': _w((lambda *s: torch.randn(*s, dtype=torch.bfloat16) * 0.1)(Fh, D))}
    _B_in = _B_setup()
    return {'A_x': _A_in['x'], 'A_w_r': _A_in['w_r'], 'A_w_i': _A_in['w_i'], 'B_x': _B_in['x'], 'B_w_in': _B_in['w_in'], 'B_w_sp': _B_in['w_sp'], 'B_b_sp': _B_in['b_sp'], 'B_w_out': _B_in['w_out']}

def reference(A_x, A_w_r, A_w_i, B_x, B_w_in, B_w_sp, B_b_sp, B_w_out):

    def _A_ref(x, w_r, w_i):
        B, S, D = x.shape
        X = (lambda x, axes=(-2, -1): torch.fft.rfft2(x.float(), dim=axes))(x, axes=(-2, -1))
        W = w_r + 1j * w_i
        X = X * W
        y = (lambda x, s, axes=(-2, -1): torch.fft.irfft2(x, s=s, dim=axes).to(torch.bfloat16))(X, (S, D), axes=(-2, -1))
        return y

    def _B_ref(x, w_in, w_sp, b_sp, w_out):
        B, S, D = x.shape
        F = w_in.shape[-1]
        Fh = F // 2
        z = _gelu(torch.einsum('bsd,df->bsf', x, w_in))
        z1 = z[..., :Fh].clone()
        z2 = _layernorm(z[..., Fh:], axis=-1)
        g = torch.einsum('ts,bsf->btf', w_sp, z2) + b_sp[None, :, None]
        return torch.einsum('bsf,fd->bsd', z1 * g, w_out)
    x = A_x
    xn = _layernorm(x, axis=-1)
    s = _A_ref(xn, A_w_r, A_w_i)
    g = _silu(_B_ref(xn, B_w_in, B_w_sp, B_b_sp, B_w_out))
    return x + s * g
```

```cpp
#include <hip/hip_runtime.h>
#include <stdint.h>

typedef unsigned v4u __attribute__((ext_vector_type(4)));
typedef unsigned v2u __attribute__((ext_vector_type(2)));
typedef float f32x4 __attribute__((ext_vector_type(4)));
typedef float f32x2 __attribute__((ext_vector_type(2)));
typedef float f32x16 __attribute__((ext_vector_type(16)));
typedef short s16x8 __attribute__((ext_vector_type(8)));
typedef short s16x4 __attribute__((ext_vector_type(4)));
typedef _Float16 h16x8 __attribute__((ext_vector_type(8)));
typedef _Float16 h16x2 __attribute__((ext_vector_type(2)));
typedef __bf16 b16x2 __attribute__((ext_vector_type(2)));
#define LAS __attribute__((address_space(3)))
#define GAS __attribute__((address_space(1)))
#define CAS __attribute__((address_space(4)))
#define DEVINL __device__ __forceinline__
#if defined(__HIP_DEVICE_COMPILE__)
#define NO_PK __attribute__((target("no-packed-fp32-ops")))
#else
#define NO_PK
#endif

#define STAMPS 0

constexpr unsigned OFF_EPOCH = 0, OFF_DBG = 128;
constexpr unsigned OFF_FA = 4096;
constexpr unsigned OFF_FY = 12288;
constexpr unsigned OFF_FW = 20480;
constexpr unsigned OFF_FZ = 24576;
constexpr unsigned OFF_FH = 32768;
constexpr unsigned OFF_ST = 65536;
constexpr unsigned OFF_Z2N = 1u << 20;
constexpr unsigned OFF_YT = 5u << 20;
constexpr unsigned OFF_WT = 7u << 20;
constexpr unsigned OFF_ZT = 9u << 20;
constexpr unsigned OFF_H = 11u << 20;
constexpr unsigned OFF_STAMP = 16u << 20;
constexpr unsigned WS_BYTES = 20u << 20;

constexpr unsigned L_F = 65536;
constexpr unsigned L_W2 = 122880;
constexpr unsigned L_FFT = 114688;
constexpr unsigned L_Z1 = 131072;
constexpr unsigned L_XR = 147456;
constexpr unsigned L_CS2 = 155648;
constexpr unsigned L_CS1 = L_CS2 + 1024;
constexpr unsigned L_XS = L_CS1 + 1024;
constexpr unsigned L_CTR = L_XS + 1024;
constexpr unsigned L_BSP = L_CTR + 256;
constexpr unsigned L_ZS = L_CTR + 1024;
constexpr unsigned L_PS = L_ZS + 1024;
constexpr unsigned LDS_BYTES = 163840;
__host__ __device__ __forceinline__ constexpr unsigned w1_base(int i) { return i < 2 ? 49152u + 8192u * i : 8192u * (i - 2); }
__host__ __device__ __forceinline__ constexpr unsigned ring_base(int i) { return i < 3 ? 65536u + 16384u * i : 16384u * (i - 3); }
constexpr unsigned RING_HI = 65536;

#define GPRIO_ 1
#define GATH 6
#define STWARM 1
#define FLAGT 17
#define YLT 24
#define A3S 27
constexpr unsigned SPIN_CAP = 1u << 14;

struct Params {
  const unsigned short* x;
  const unsigned short* wr;
  const unsigned short* wi;
  const unsigned short* win;
  const unsigned short* wsp;
  const unsigned short* bsp;
  const unsigned short* wout;
  unsigned short* out;
  unsigned char* ws;
};

template <int V> struct IC { static constexpr int value = V; };
template <int I, int N, class F> DEVINL void static_for(F&& f) {
  if constexpr (I < N) {
    f(IC<I>{});
    static_for<I + 1, N>(f);
  }
}
DEVINL v4u make_rsrc(const void* p, unsigned bytes) {
  const unsigned long long a = (unsigned long long)p;
  v4u r;
  r[0] = __builtin_amdgcn_readfirstlane((unsigned)a);
  r[1] = __builtin_amdgcn_readfirstlane((unsigned)(a >> 32) & 0xffffu);
  r[2] = bytes;
  r[3] = 0x00020000u;
  return r;
}
DEVINL void dma16(v4u rs, unsigned voff, unsigned soff, unsigned lds) {
  asm volatile("s_mov_b32 m0, %3\n\ts_nop 0\n\tbuffer_load_dwordx4 %0, %1, %2 offen lds" ::"v"(voff), "s"(rs), "s"(soff), "s"(lds) : "memory");
}
DEVINL void dma16x2(v4u rs, unsigned v0, unsigned v1, unsigned soff, unsigned lds) {
  asm volatile("s_mov_b32 m0, %4\n\ts_nop 0\n\tbuffer_load_dwordx4 %0, %2, %3 offen lds\n\tbuffer_load_dwordx4 %1, %2, %3 offen offset:1024 lds" ::"v"(v0),
               "v"(v1), "s"(rs), "s"(soff), "s"(lds)
               : "memory");
}
DEVINL void dma16x4(v4u rs, unsigned v0, unsigned v1, unsigned v2, unsigned v3, unsigned soff, unsigned lds) {
  asm volatile(
      "s_mov_b32 m0, %6\n\ts_nop 0\n\tbuffer_load_dwordx4 %0, %4, %5 offen lds\n\tbuffer_load_dwordx4 %1, %4, %5 offen offset:1024 lds\n\t"
      "buffer_load_dwordx4 %2, %4, %5 offen offset:2048 lds\n\tbuffer_load_dwordx4 %3, %4, %5 offen offset:3072 lds" ::"v"(v0),
      "v"(v1), "v"(v2), "v"(v3), "s"(rs), "s"(soff), "s"(lds)
      : "memory");
}
DEVINL void set_m0(unsigned lds) { asm volatile("s_mov_b32 m0, %0\n\ts_nop 0" ::"s"(lds) : "memory"); }
template <int IMM> DEVINL void dma_piece(v4u rs, unsigned voff, unsigned soff) {
  asm volatile("buffer_load_dwordx4 %0, %1, %2 offen offset:%3 lds" ::"v"(voff), "s"(rs), "s"(soff), "i"(IMM) : "memory");
}
DEVINL void dma4(v4u rs, unsigned voff, unsigned soff, unsigned lds) {
  asm volatile("s_mov_b32 m0, %3\n\ts_nop 0\n\tbuffer_load_dword %0, %1, %2 offen lds" ::"v"(voff), "s"(rs), "s"(soff), "s"(lds) : "memory");
}
template <int IMM> DEVINL void ld16_sc1(v4u& d, v4u rs, unsigned voff, unsigned soff) {
  asm volatile("buffer_load_dwordx4 %0, %1, %2, %3 offen offset:%4 sc1" : "=v"(d) : "v"(voff), "s"(rs), "s"(soff), "i"(IMM) : "memory");
}
template <int IMM> DEVINL void ld4_sc1(unsigned& d, v4u rs, unsigned voff, unsigned soff) {
  asm volatile("buffer_load_dword %0, %1, %2, %3 offen offset:%4 sc1" : "=v"(d) : "v"(voff), "s"(rs), "s"(soff), "i"(IMM) : "memory");
}
template <int IMM> DEVINL void st16_sc1(v4u d, v4u rs, unsigned voff, unsigned soff) {
  asm volatile("buffer_store_dwordx4 %0, %1, %2, %3 offen offset:%4 sc1\n\ts_nop 1" ::"v"(d), "v"(voff), "s"(rs), "s"(soff), "i"(IMM) : "memory");
}
template <int IMM> DEVINL void st8_sc1(v2u d, v4u rs, unsigned voff, unsigned soff) {
  asm volatile("buffer_store_dwordx2 %0, %1, %2, %3 offen offset:%4 sc1" ::"v"(d), "v"(voff), "s"(rs), "s"(soff), "i"(IMM) : "memory");
}
template <int IMM> DEVINL void st4_sc1(unsigned d, v4u rs, unsigned voff, unsigned soff) {
  asm volatile("buffer_store_dword %0, %1, %2, %3 offen offset:%4 sc1" ::"v"(d), "v"(voff), "s"(rs), "s"(soff), "i"(IMM) : "memory");
}
template <int IMM> DEVINL void st2_sc1(unsigned d, v4u rs, unsigned voff, unsigned soff) {
  asm volatile("buffer_store_short %0, %1, %2, %3 offen offset:%4 sc1" ::"v"(d), "v"(voff), "s"(rs), "s"(soff), "i"(IMM) : "memory");
}
template <int N> DEVINL void wait_vm() { asm volatile("s_waitcnt vmcnt(%0)" ::"i"(N) : "memory"); }
DEVINL void bar() {
  asm volatile("s_waitcnt lgkmcnt(0)" ::: "memory");
  __builtin_amdgcn_s_barrier();
  asm volatile("" ::: "memory");
}
#define SCHED() __builtin_amdgcn_sched_barrier(0)
#define PIN(x) asm volatile("" : "+v"(x))
#define FENCE() asm volatile("" ::: "memory")

DEVINL f32x16 mfma32(v4u a, v4u b, f32x16 c) {
  return __builtin_amdgcn_mfma_f32_32x32x16_bf16(__builtin_bit_cast(s16x8, a), __builtin_bit_cast(s16x8, b), c, 0, 0, 0);
}
DEVINL f32x4 mfma16(v4u a, v4u b, f32x4 c) {
  return __builtin_amdgcn_mfma_f32_16x16x32_bf16(__builtin_bit_cast(s16x8, a), __builtin_bit_cast(s16x8, b), c, 0, 0, 0);
}
DEVINL f32x4 mfma_h(v4u a, v4u b, f32x4 c) {
  return __builtin_amdgcn_mfma_f32_16x16x32_f16(__builtin_bit_cast(h16x8, a), __builtin_bit_cast(h16x8, b), c, 0, 0, 0);
}
DEVINL f32x16 splat16(float v) { return f32x16{v, v, v, v, v, v, v, v, v, v, v, v, v, v, v, v}; }
DEVINL unsigned pk_bf(float a, float b) { return __builtin_bit_cast(unsigned, __builtin_convertvector((f32x2{a, b}), b16x2)); }
DEVINL unsigned pk_h(float a, float b) { return __builtin_bit_cast(unsigned, __builtin_convertvector((f32x2{a, b}), h16x2)); }
DEVINL float asf(unsigned u) { return __builtin_bit_cast(float, u); }
DEVINL unsigned asu(float f) { return __builtin_bit_cast(unsigned, f); }
DEVINL float bf_lo(unsigned u) { return __builtin_bit_cast(float, u << 16); }
DEVINL float bf_hi(unsigned u) { return __builtin_bit_cast(float, u & 0xffff0000u); }
template <class H> DEVINL void gelu16(f32x16& t, float k1, float k2, float sg, H&& hook) {
  float u[16];
#pragma unroll
  for (int e = 0; e < 16; ++e) u[e] = t[e] * t[e];
#pragma unroll
  for (int e = 0; e < 16; ++e) u[e] = __builtin_fmaf(u[e], k2, k1);
  SCHED();
#pragma unroll
  for (int e = 0; e < 16; ++e) u[e] = u[e] * t[e];
  SCHED();
  hook(IC<0>{});
  SCHED();
#pragma unroll
  for (int e = 0; e < 16; ++e) u[e] = __builtin_amdgcn_exp2f(u[e]);
  SCHED();
#pragma unroll
  for (int e = 0; e < 16; ++e) u[e] = __builtin_fmaf(u[e], sg, sg);
  SCHED();
  hook(IC<1>{});
  SCHED();
#pragma unroll
  for (int e = 0; e < 16; ++e) u[e] = __builtin_amdgcn_rcpf(u[e]);
  SCHED();
#pragma unroll
  for (int e = 0; e < 16; ++e) t[e] = t[e] * u[e];
}
DEVINL v4u tr_frag(LAS unsigned char* L, unsigned a0, unsigned a1) {
  const s16x4 lo = __builtin_amdgcn_ds_read_tr16_b64_v4i16((LAS s16x4*)(L + a0));
  const s16x4 hi = __builtin_amdgcn_ds_read_tr16_b64_v4i16((LAS s16x4*)(L + a1));
  const v2u l2 = __builtin_bit_cast(v2u, lo), h2 = __builtin_bit_cast(v2u, hi);
  return v4u{l2[0], l2[1], h2[0], h2[1]};
}
DEVINL bool wave_all(bool c) { return __builtin_amdgcn_ballot_w64(!c) == 0ull; }
DEVINL float half_sum(float v) {
  const unsigned u = asu(v);
  const auto r = __builtin_amdgcn_permlane32_swap(u, u, false, false);
  const unsigned lo = r[0], hi = r[1];
  return asf(lo) + asf(hi);
}
DEVINL float row_pair_sum(float v) {
  const unsigned u = asu(v);
  const auto r = __builtin_amdgcn_permlane16_swap(u, u, false, false);
  const unsigned lo = r[0], hi = r[1];
  return asf(lo) + asf(hi);
}
DEVINL unsigned fresh_lane() {
  unsigned l;
  asm volatile("v_mbcnt_lo_u32_b32 %0, -1, 0\n\tv_mbcnt_hi_u32_b32 %0, -1, %0" : "=v"(l));
  return l;
}
DEVINL float rev448(float a, float b) {
  const float pr = a * b;
  const float q = __builtin_floorf(pr * (1.0f / 448.0f));
  return __builtin_fmaf(q, -448.0f, pr) * (1.0f / 448.0f);
}

template <int SLEEP> DEVINL void poll_flags(v4u wsr, unsigned voff, bool active, unsigned tag, unsigned code) {
#pragma unroll 1
  for (unsigned spins = 0;; ++spins) {
    unsigned f;
    asm volatile("buffer_load_dword %0, %1, %2, 0 offen sc1\n\ts_waitcnt vmcnt(0)" : "=v"(f) : "v"(voff), "s"(wsr) : "memory");
    if (wave_all(!active || f == tag)) break;
    if (spins > SPIN_CAP) {
      st4_sc1<0>(code, wsr, OFF_DBG, 0);
      break;
    }
    if (SLEEP) __builtin_amdgcn_s_sleep(SLEEP);
  }
}
DEVINL void grp_arrive(LAS unsigned char* L, unsigned ctr) {
  const unsigned a = (unsigned)(unsigned long long)L + ctr;
  asm volatile("s_mov_b64 exec, 1\n\tds_add_u32 %0, %1\n\ts_mov_b64 exec, -1" ::"v"(a), "v"(1u) : "memory");
}
DEVINL void grp_wait(LAS unsigned char* L, unsigned ctr, unsigned count) {
  const unsigned a = (unsigned)(unsigned long long)L + ctr;
#pragma unroll 1
  for (unsigned spins = 0; spins < (1u << 22); ++spins) {
    unsigned v;
    asm volatile("ds_read_b32 %0, %1\n\ts_waitcnt lgkmcnt(0)" : "=v"(v) : "v"(a) : "memory");
    if ((unsigned)__builtin_amdgcn_readfirstlane(v) >= count) break;
  }
}
DEVINL void grp_wait_sleep(LAS unsigned char* L, unsigned ctr, unsigned count) {
  const unsigned a = (unsigned)(unsigned long long)L + ctr;
#pragma unroll 1
  for (unsigned spins = 0; spins < (1u << 20); ++spins) {
    unsigned v;
    asm volatile("ds_read_b32 %0, %1\n\ts_waitcnt lgkmcnt(0)" : "=v"(v) : "v"(a) : "memory");
    if ((unsigned)__builtin_amdgcn_readfirstlane(v) >= count) break;
    __builtin_amdgcn_s_sleep(2);
  }
}
DEVINL void grp_sync(LAS unsigned char* L, unsigned ctr, unsigned count) {
  grp_arrive(L, ctr);
  grp_wait(L, ctr, count);
}
DEVINL bool grp_arrive_last(LAS unsigned char* L, unsigned ctr, unsigned total) {
  const unsigned a = (unsigned)(unsigned long long)L + ctr;
  unsigned v = 1u;
  asm volatile("s_mov_b64 exec, 1\n\tds_add_rtn_u32 %0, %1, %0\n\ts_waitcnt lgkmcnt(0)\n\ts_mov_b64 exec, -1" : "+v"(v) : "v"(a) : "memory");
  return (unsigned)__builtin_amdgcn_readfirstlane(v) == total - 1;
}

#define STAMP(i) do {} while (0)
#define STAMPX(set, i) do {} while (0)
#define STAMP_OUT() do {} while (0)

struct cf { float x, y; };
DEVINL cf cadd(cf a, cf b) { return cf{a.x + b.x, a.y + b.y}; }
DEVINL cf csub(cf a, cf b) { return cf{a.x - b.x, a.y - b.y}; }
template <int SG> DEVINL cf rot90(cf a) { return SG < 0 ? cf{a.y, -a.x} : cf{-a.y, a.x}; }
template <int SG> DEVINL cf ctw(cf a, cf w) {
  return SG < 0 ? cf{a.x * w.x + a.y * w.y, a.y * w.x - a.x * w.y} : cf{a.x * w.x - a.y * w.y, a.y * w.x + a.x * w.y};
}
template <int SG> DEVINL void dft4(cf& a0, cf& a1, cf& a2, cf& a3) {
  const cf s0 = cadd(a0, a2), d0 = csub(a0, a2), s1 = cadd(a1, a3), d1 = rot90<SG>(csub(a1, a3));
  a0 = cadd(s0, s1); a2 = csub(s0, s1); a1 = cadd(d0, d1); a3 = csub(d0, d1);
}
template <int SG> DEVINL void dft8(cf (&a)[8]) {
  cf e0 = a[0], e1 = a[2], e2 = a[4], e3 = a[6], o0 = a[1], o1 = a[3], o2 = a[5], o3 = a[7];
  dft4<SG>(e0, e1, e2, e3);
  dft4<SG>(o0, o1, o2, o3);
  const float h = 0.70710678118654752f;
  const cf r1 = rot90<SG>(o1), r3 = rot90<SG>(o3);
  o1 = cf{o1.x + r1.x, o1.y + r1.y};
  o2 = rot90<SG>(o2);
  o3 = cf{r3.x - o3.x, r3.y - o3.y};
  a[0] = cadd(e0, o0); a[4] = csub(e0, o0);
  a[1] = cf{__builtin_fmaf(o1.x, h, e1.x), __builtin_fmaf(o1.y, h, e1.y)}; a[5] = cf{__builtin_fmaf(o1.x, -h, e1.x), __builtin_fmaf(o1.y, -h, e1.y)};
  a[2] = cadd(e2, o2); a[6] = csub(e2, o2);
  a[3] = cf{__builtin_fmaf(o3.x, h, e3.x), __builtin_fmaf(o3.y, h, e3.y)}; a[7] = cf{__builtin_fmaf(o3.x, -h, e3.x), __builtin_fmaf(o3.y, -h, e3.y)};
}
DEVINL unsigned fsw(unsigned i) { return i ^ (((i >> 4) & 7u) | (((i >> 6) & 1u) << 3)); }
DEVINL void lds_wc(LAS unsigned char* L, unsigned a, cf v) { *(LAS f32x2*)(L + a) = f32x2{v.x, v.y}; }
DEVINL cf lds_rc(LAS unsigned char* L, unsigned a) { const f32x2 v = *(LAS f32x2*)(L + a); return cf{v[0], v[1]}; }
struct FftAddr { unsigned rd[2], wa[8], wb[8]; };
template <int SG> DEVINL void fft512(cf (&v)[8], const cf (&tb)[7], const cf (&tc)[7], LAS unsigned char* L, const FftAddr& A) {
  dft8<SG>(v);
#pragma unroll
  for (int r = 0; r < 8; ++r) lds_wc(L, A.wa[r], v[r]);
  FENCE();
#pragma unroll
  for (int r = 0; r < 8; ++r) v[r] = lds_rc(L, A.rd[r & 1] + 512 * r);
  FENCE();
#pragma unroll
  for (int r = 1; r < 8; ++r) v[r] = ctw<SG>(v[r], tb[r - 1]);
  dft8<SG>(v);
#pragma unroll
  for (int r = 0; r < 8; ++r) lds_wc(L, A.wb[r], v[r]);
  FENCE();
#pragma unroll
  for (int r = 0; r < 8; ++r) v[r] = lds_rc(L, A.rd[r & 1] + 512 * r);
  FENCE();
#pragma unroll
  for (int r = 1; r < 8; ++r) v[r] = ctw<SG>(v[r], tc[r - 1]);
  dft8<SG>(v);
}

constexpr int p2_dma_in(int s) { return (s >= 1 && s + 6 < 32) ? 4 : 0; }
constexpr int p2_b_in(int s) { return (s >= 5 && ((s - 1) & 3) < 2 && ((s - 1) >> 2) + 1 < 8) ? 4 : 0; }
constexpr int p2_pos(int step) {
  int pos = 28 + 16;
  for (int s = 0; s < step; ++s) pos += p2_dma_in(s) + p2_b_in(s);
  return pos;
}
constexpr int p2_dma_done(int u) { return u < 3 ? 4 * (u + 1) : u < 7 ? 4 * (u + 1) + 16 : p2_pos(u - 5) - (p2_b_in(u - 6) ? 1 : 0); }
constexpr int p2_b_done(int c) { return c < 2 ? 12 + 8 * (c + 1) : p2_pos(4 * (c - 1) + 3); }

__global__ __launch_bounds__(512, 2) NO_PK void fused_kernel(Params P) {
  __shared__ __attribute__((aligned(16))) unsigned char smem[LDS_BYTES];
  LAS unsigned char* L = (LAS unsigned char*)smem;
  const unsigned lbase = (unsigned)(unsigned long long)L;
  int tid = __builtin_amdgcn_workitem_id_x();
  unsigned lane = tid & 63;
  const int w = __builtin_amdgcn_readfirstlane(tid >> 6);
  STAMP(0);
  unsigned pfd;
  {
    unsigned long long pc;
    unsigned csz;
    asm volatile("s_getpc_b64 %0\ncode_begin_lbl:\n\ts_mov_b32 %1, (code_end_lbl - code_begin_lbl)" : "=s"(pc), "=s"(csz));
    const unsigned o = (unsigned)tid * 128u;
    const unsigned oc = o < csz ? o : 0u;
    asm volatile("global_load_dword %0, %1, %2" : "=v"(pfd) : "v"(oc), "s"(pc) : "memory");
  }
  const int bid = __builtin_amdgcn_workgroup_id_x();
  const int xcd = bid & 7, rr = bid >> 3;
  const bool is_main = rr < 28;
  const int p = 4 * (xcd >> 1) + (rr & 3);
  const int j = 7 * (xcd & 1) + (rr >> 2);
  const int sidx = xcd * 4 + (rr - 28);
  const v4u wsr = make_rsrc(P.ws, WS_BYTES);
  unsigned tag;
  unsigned h2 = lane >> 5, r32 = lane & 31;
  unsigned trq = (lane & 15) >> 2, trp = lane & 3, trb = (lane >> 4) & 1;
#define RELANE() do { lane = fresh_lane(); tid = w * 64 + lane; h2 = lane >> 5; r32 = lane & 31; trq = (lane & 15) >> 2; trp = lane & 3; trb = (lane >> 4) & 1; } while (0)
#define GET_TAG(ev) do { unsigned e_; asm volatile("v_readlane_b32 %0, %1, 63" : "=s"(e_) : "v"(ev), "v"(pfd) : "memory"); tag = e_ * 1664525u + 1013904223u; } while (0)

  f32x16 a1 = splat16(0.f);
  float gk1 = 0.f, gk2 = 0.f, gsg = 0.f;
  auto finish_z1 = [&](auto&& hook) __attribute__((always_inline)) {
    gelu16(a1, gk1, gk2, gsg, hook);
    v4u o[2];
#pragma unroll
    for (int e = 0; e < 8; ++e) o[e >> 2][e & 3] = pk_bf(a1[2 * e], a1[2 * e + 1]);
    *(LAS v4u*)(L + L_Z1 + w * 2048 + lane * 16) = o[0];
    *(LAS v4u*)(L + L_Z1 + w * 2048 + 1024 + lane * 16) = o[1];
  };
  v4u wq[2];
  unsigned yin[8];
  const int slot = is_main ? 16 * j + p : 0;
  const int kw = is_main ? slot : 224;
  const unsigned fft_flags = lane < 32 ? OFF_FW + lane * 32u : OFF_FY + (unsigned)(is_main ? j : 0) * 512u + (lane & 15) * 32u;
  auto load_fft_in = [&]() __attribute__((always_inline)) {
    const unsigned so = OFF_YT + (unsigned)slot * 8192u, vo = (tid - 256) * 4u;
    ld4_sc1<0>(yin[0], wsr, vo, so); ld4_sc1<1024>(yin[1], wsr, vo, so); ld4_sc1<2048>(yin[2], wsr, vo, so); ld4_sc1<3072>(yin[3], wsr, vo, so);
    ld4_sc1<0>(yin[4], wsr, vo, so + 4096); ld4_sc1<1024>(yin[5], wsr, vo, so + 4096); ld4_sc1<2048>(yin[6], wsr, vo, so + 4096); ld4_sc1<3072>(yin[7], wsr, vo, so + 4096);
    ld16_sc1<0>(wq[0], wsr, lane * 16u, OFF_WT + (unsigned)(kw * 4 + w - 4) * 2048u);
    ld16_sc1<1024>(wq[1], wsr, lane * 16u, OFF_WT + (unsigned)(kw * 4 + w - 4) * 2048u);
  };
#define FFT_IN_LANDED() do { wait_vm<0>(); asm volatile("" : "+v"(yin[0]), "+v"(yin[1]), "+v"(yin[2]), "+v"(yin[3]), "+v"(yin[4]), "+v"(yin[5]), "+v"(yin[6]), "+v"(yin[7]), "+v"(wq[0]), "+v"(wq[1])::"memory"); } while (0)
  const int th = j / 7, dt = j % 7;
  const v4u xr = make_rsrc(P.x, 2048u * 896u);

  if (__builtin_expect(is_main, 1)) {
    const int wm = w & 3, wn = w >> 2;
    const v4u wir = make_rsrc(P.win, 448u * 3584u);
    const int c0 = (j + 2 * p) % 7;
    const unsigned ctab = (unsigned)(0x5432106543210ull >> (4 * c0));
    const unsigned xv0 = (unsigned)(128 * p + 16 * w + (lane >> 3)) * 896u + (((lane & 7) ^ (lane >> 4)) << 4);
    const unsigned xv1 = (xv0 ^ 64u) + 8u * 896u - 1024u;
    const unsigned wv = (unsigned)(8 * w + (lane >> 3)) * 3584u + (unsigned)j * 128u + (((lane & 7) ^ (((lane >> 4) & 1) << 2)) << 4);
    auto issue_x = [&](int i) __attribute__((always_inline)) { dma16x2(xr, xv0, xv1, ((ctab >> (4 * i)) & 7u) * 128u, lbase + (i & 3) * 16384 + w * 2048); };
    auto issue_w2 = [&](int i) __attribute__((always_inline)) { dma16(wir, wv, ((ctab >> (4 * i)) & 7u) * (64u * 3584u) + 1792u, lbase + L_W2 + (i & 3) * 8192 + w * 1024); };
    auto issue_w1 = [&](int i) __attribute__((always_inline)) { dma16(wir, wv, ((ctab >> (4 * i)) & 7u) * (64u * 3584u), lbase + w1_base(i) + w * 1024); };
    STAMPX(3, 1);
    issue_x(0); issue_w2(0);
    STAMPX(3, 2);
    issue_x(1); issue_w2(1);
    STAMPX(3, 3);
    if (tid < 16) *(LAS unsigned*)(L + L_CTR + 4 * tid) = 0u;

    {
      unsigned k = 16 * j + 8 * wn + ((lane & 15) >> 1);
      float ph = (lane & 1) ? 0.25f : 0.0f;
      if (k == 0 && (lane & 1)) { k = 224; ph = 0.0f; }
      const float kf = (float)k, fs = kf * (1.0f / 448.0f), dl = (float)(8 * (lane >> 4) + 32 * (wm & 1));
      const unsigned fo = L_F + (wn * 28 + 2 * wm + (lane >> 5)) * 1024 + (((lane >> 4) & 1) * 32 + (lane & 15)) * 16;
#pragma unroll
      for (int a = 0; a < 4; ++a) {
        if (a < 3 || wm < 2) {
          const unsigned cc = (ctab >> (4 * (2 * a + (wm >> 1)))) & 7u;
          const float fa = rev448(kf, dl + (float)(64 * cc)) + ph;
          float cv[8];
#pragma unroll
          for (int e = 0; e < 8; ++e) cv[e] = __builtin_amdgcn_cosf(__builtin_fmaf((float)e, fs, fa));
          SCHED();
          if (a == 0) issue_x(2);
          v4u fh, fl;
#pragma unroll
          for (int e = 0; e < 4; ++e) {
            const unsigned h0 = asu(cv[2 * e]) & 0xffff0000u, h1 = asu(cv[2 * e + 1]) & 0xffff0000u;
            fh[e] = __builtin_amdgcn_perm(h1, h0, 0x07060302u);
            fl[e] = pk_bf(cv[2 * e] - asf(h0), cv[2 * e + 1] - asf(h1));
          }
          *(LAS v4u*)(L + fo + a * 8192) = fh;
          *(LAS v4u*)(L + fo + a * 8192 + 256) = fl;
          SCHED();
          if (a == 0) issue_w2(2);
        }
        STAMPX(3, 4 + a);
      }
    }
    dma4(make_rsrc(P.bsp, 4096u), lane * 4u, (unsigned)p * 256u, lbase + L_BSP);
    unsigned epoch_v;
    {
      const unsigned wb_ = w == 0 ? (STWARM ? OFF_ST + (unsigned)(p * 14 + j) * 2048u : 0u) : w == 1 ? OFF_FA + (unsigned)j * 512u + (lane & 4) * (OFF_FY - OFF_FA) / 4 : w == 2 ? OFF_FH + (unsigned)p * 512u : w == 3 ? OFF_FW : OFF_FZ + (unsigned)(w - 4) * 1024u;
      const unsigned wm_ = w == 0 ? (STWARM ? 15u : 0u) : w < 3 ? 3u : 7u;
      const unsigned eo = lane == 63 ? 0u : wb_ + (lane & wm_) * 128u;
      asm volatile("buffer_load_dword %0, %1, %2, 0 offen sc1" : "=v"(epoch_v) : "v"(eo), "s"(wsr) : "memory");
    }
#define NSMALL 2
    STAMP(1);

    unsigned aoff[4], boff[4];
#pragma unroll
    for (int r = 0; r < 4; ++r) {
      aoff[r] = (32 * wm + r32) * 128 + (((2 * (r ^ wm) + h2) ^ ((r32 >> 1) & 7)) << 4);
      boff[r] = L_W2 + (16 * (r ^ wm) + 8 * h2 + trq) * 128 + ((wn ^ (trq >> 1)) << 6) + 32 * trb + 8 * trp;
      PIN(aoff[r]);
      PIN(boff[r]);
    }
    unsigned soff[2];
    soff[0] = (16 * w + (lane & 15)) * 128 + (((lane >> 4) ^ ((lane & 15) >> 1)) << 4);
    soff[1] = soff[0] ^ 64u;
    PIN(soff[0]); PIN(soff[1]);
    v4u xa[7][4];
    v4u wb[2][4], xs[2][2];
    f32x16 a2 = splat16(0.f);
    f32x4 sx1 = f32x4{0.f, 0.f, 0.f, 0.f}, sx2 = sx1, scs = sx1;
    v4u ones = v4u{0x3f803f80u, 0x3f803f80u, 0x3f803f80u, 0x3f803f80u};
    const unsigned selv = ((lane & 15) == ((lane >> 4) & 1)) ? 0x3f803f80u : 0u;
    v4u sel = v4u{selv, selv, selv, selv};
    PIN(ones); PIN(sel);
    const unsigned rowx = L_XS + (32 * wm + r32) * 8;
    const unsigned colw = (32 * wn + (lane & 15)) * 16 + wm * 4, colr = (32 * wn + r32) * 16 + h2 * 8;
#pragma unroll
    for (int i = 0; i < 7; ++i) {
      const int c = i - 1;
      if (i < 3) wait_vm<6 + NSMALL>(); else if (i < 5) wait_vm<6>(); else if (i == 5) wait_vm<5>(); else wait_vm<4>();
      STAMPX(2, 1 + 2 * i);
      bar();
      STAMPX(2, 2 + 2 * i);
      if (i == 0) { STAMP(2); STAMPX(3, 9); }
      if (i == 3) GET_TAG(epoch_v);
#pragma unroll
      for (int r = 0; r < 4; ++r) {
        SCHED();
        if (i >= 1) a2 = mfma32(wb[c & 1][r], xa[c][r], a2);
        SCHED();
        if (r < 2) xs[i & 1][r] = *(LAS v4u*)(L + soff[r] + (i & 3) * 16384);
        wb[i & 1][r] = tr_frag(L, boff[r] + (i & 3) * 8192, boff[r] + (i & 3) * 8192 + 512);
        SCHED();
        if (i >= 1) {
          if (r & 1) sx2 = mfma16(xs[c & 1][r >> 1], xs[c & 1][r >> 1], sx2); else sx1 = mfma16(ones, xs[c & 1][r >> 1], sx1);
          if (r == 3) scs = mfma16(sel, wb[c & 1][0], scs);
        }
        SCHED();
        xa[i][r] = *(LAS v4u*)(L + aoff[r] + (i & 3) * 16384);
        SCHED();
        if (i <= 3) { if (r == 0) issue_x(i + 3); if (r == 2) issue_w2(i + 3); }
        if (i >= 4 && (r & 1)) issue_w1(2 * (i - 4) + (r >> 1));
      }
      SCHED();
    }
    sx1 = mfma16(ones, xs[0][0], sx1);
    sx2 = mfma16(xs[0][0], xs[0][0], sx2);
    scs = mfma16(sel, wb[0][0], scs);
    sx1 = mfma16(ones, xs[0][1], sx1);
    sx2 = mfma16(xs[0][1], xs[0][1], sx2);
    SCHED();
    a2 = mfma32(wb[0][0], xa[6][0], a2);
    a2 = mfma32(wb[0][1], xa[6][1], a2);
    SCHED();
    {
      const float d01 = (lane & 1) ? sx2[1] : sx2[0], d23 = (lane & 1) ? sx2[3] : sx2[2];
      const float dg = (lane & 2) ? d23 : d01;
      const float nm = sx1[0] * (-1.0f / 448.0f);
      const float ve = __builtin_fmaxf(dg * (1.0f / 448.0f) - nm * nm, 0.f) + 1e-5f;
      if (((lane & 15) >> 2) == (lane >> 4)) *(LAS f32x2*)(L + L_XS + (16 * w + (lane & 15)) * 8) = f32x2{nm, ve};
      if (lane < 16) {
        *(LAS float*)(L + L_CS2 + colw) = scs[0];
        *(LAS float*)(L + L_CS2 + colw + 256) = scs[1];
      }
    }
    SCHED();
    a2 = mfma32(wb[0][2], xa[6][2], a2);
    a2 = mfma32(wb[0][3], xa[6][3], a2);
    SCHED();
    STAMP(3);
    STAMPX(2, 15);
    bar();
    float nmu, rho;
    unsigned fv[4];
#pragma unroll
    for (int r = 0; r < 4; ++r) fv[r] = L_F + wn * 28672 + lane * 16 + (r ^ wm) * 1024;
    {
      const f32x2 t0 = *(LAS f32x2*)(L + rowx);
      const f32x2 c2 = *(LAS f32x2*)(L + L_CS2 + colr);
      issue_w1(6);
#pragma unroll
      for (int r = 0; r < 4; ++r) wb[0][r] = *(LAS v4u*)(L + fv[r]);
      nmu = t0[0];
      a2 = __builtin_amdgcn_mfma_f32_32x32x2f32(c2[0] + c2[1], nmu, a2, 0, 0, 0);
      const float ve = t0[1];
      rho = __builtin_amdgcn_rsqf(ve);
      gsg = ve * rho;
      gk1 = -2.302208198f * rho;
      gk2 = -0.1029432397f * rho * rho * rho;
      gelu16(a2, gk1, gk2, gsg, [](auto) {});
      float s1a = a2[0], s1b = a2[1], s2a = a2[0] * a2[0], s2b = a2[1] * a2[1];
#pragma unroll
      for (int e = 2; e < 16; e += 2) {
        s1a += a2[e]; s1b += a2[e + 1];
        s2a = __builtin_fmaf(a2[e], a2[e], s2a); s2b = __builtin_fmaf(a2[e + 1], a2[e + 1], s2b);
      }
      const auto sw = __builtin_amdgcn_permlane32_swap(asu(s1a + s1b), asu(s2a + s2b), false, false);
      const unsigned sw0 = sw[0], sw1 = sw[1];
      const float part = asf(sw0) + asf(sw1);
      if (wn) *(LAS float*)(L + L_PS + wm * 256 + lane * 4) = part;
      bar();
      if (!wn) {
        const float tot = part + *(LAS float*)(L + L_PS + wm * 256 + lane * 4);
        st8_sc1<0>(v2u{asu(tot), tag}, wsr, r32 * 16u + h2 * 8u, OFF_ST + (unsigned)((p * 14 + j) * 128 + 32 * wm) * 16u);
      }
    }
    STAMP(4);
    const unsigned gso = OFF_ST + (unsigned)(p * 14) * 2048u;
    const unsigned gvo = (lane >> 4) * 2048u + (16 * w + (lane & 15)) * 16u, gv3 = gvo & ~4096u;
    v4u gq[4];
    auto issue_gather = [&]() __attribute__((always_inline)) {
#pragma unroll
      for (int a = 0; a < 4; ++a) ld16_sc1<0>(gq[a], wsr, a == 3 ? gv3 : gvo, gso + a * 8192);
    };
    unsigned yq[4];
    {
      f32x16 ay = splat16(0.f);
#pragma unroll
      for (int i = 1; i <= 7; ++i) {
#pragma unroll
        for (int r = 0; r < 4; ++r) {
          SCHED();
          ay = mfma32(wb[(i - 1) & 1][r], xa[i - 1][r], ay);
          SCHED();
          if (i < 7) wb[i & 1][r] = *(LAS v4u*)(L + fv[r] + i * 4096);
          if (i == GATH) ld16_sc1<0>(gq[r], wsr, r == 3 ? gv3 : gvo, gso + r * 8192);
        }
        SCHED();
      }
      const float dc = (j == 0 && wn == 0 && h2 == 0) ? 448.0f * nmu : 0.0f;
      yq[0] = pk_h((ay[0] + ay[8] + dc) * rho, (ay[1] + ay[9]) * rho);
#pragma unroll
      for (int q = 1; q < 4; ++q) yq[q] = pk_h((ay[2 * q] + ay[2 * q + 8]) * rho, (ay[2 * q + 1] + ay[2 * q + 9]) * rho);
      const unsigned yso = OFF_YT + (unsigned)(16 * j + 8 * wn) * 8192u + (unsigned)(128 * p + 32 * wm) * 4u;
      const unsigned yvo = h2 * 16384u + r32 * 4u;
      st4_sc1<0>(yq[0], wsr, yvo, yso);
      st4_sc1<0>(yq[1], wsr, yvo, yso + 8192);
      st4_sc1<0>(yq[2], wsr, yvo, yso + 32768);
      st4_sc1<0>(yq[3], wsr, yvo, yso + 40960);
    }
    STAMP(5);
    {
      wait_vm<4>();
#pragma unroll 1
      for (unsigned spins = 0;; ++spins) {
        asm volatile("" : "+v"(gq[0]), "+v"(gq[1]), "+v"(gq[2]), "+v"(gq[3])::"memory");
        unsigned bad = 0;
#pragma unroll
        for (int a = 0; a < 4; ++a) bad |= (gq[a][1] ^ tag) | (gq[a][3] ^ tag);
        if (wave_all(bad == 0)) break;
        if (spins > SPIN_CAP) { st4_sc1<0>(1u, wsr, OFF_DBG, 0); break; }
        issue_gather();
        wait_vm<0>();
      }
      STAMPX(1, 14);
      const float s1 = (asf(gq[0][0]) + asf(gq[1][0])) + (asf(gq[2][0]) + (lane < 32 ? asf(gq[3][0]) : 0.f));
      const float s2 = (asf(gq[0][2]) + asf(gq[1][2])) + (asf(gq[2][2]) + (lane < 32 ? asf(gq[3][2]) : 0.f));
      const auto sw = __builtin_amdgcn_permlane32_swap(asu(s1), asu(s2), false, false);
      const unsigned sw0 = sw[0], sw1 = sw[1];
      const float tot = row_pair_sum(asf(sw0) + asf(sw1));
      *(LAS float*)(L + L_ZS + (16 * w + (lane & 15)) * 8 + h2 * 4) = tot;
    }
    bar();
    {
      const f32x2 t0 = *(LAS f32x2*)(L + rowx + (L_ZS - L_XS));
      const float mean = t0[0] * (1.0f / 896.0f);
      const float rs = __builtin_amdgcn_rsqf(__builtin_fmaxf(t0[1] * (1.0f / 896.0f) - mean * mean, 0.f) + 1e-5f);
      const float mo = -mean * rs;
      const unsigned tw = L_Z1 + w * 2048 + r32 * 64;
#pragma unroll
      for (int g = 0; g < 4; ++g) {
        v2u o;
        o[0] = pk_bf(__builtin_fmaf(a2[4 * g], rs, mo), __builtin_fmaf(a2[4 * g + 1], rs, mo));
        o[1] = pk_bf(__builtin_fmaf(a2[4 * g + 2], rs, mo), __builtin_fmaf(a2[4 * g + 3], rs, mo));
        *(LAS v2u*)(L + tw + (((2 * g + h2) ^ ((r32 >> 1) & 7)) << 3)) = o;
      }
      FENCE();
      const unsigned tr0 = L_Z1 + w * 2048 + (8 * h2 + trq) * 64 + (((4 * trb + trp) ^ (4 * h2 + (trq >> 1))) << 3);
      const unsigned tr1 = (tr0 ^ 16u) + 256;
      const v4u z0 = tr_frag(L, tr0, tr1), z1f = tr_frag(L, tr0 + 1024, tr1 + 1024);
      const unsigned zso = OFF_Z2N + (unsigned)((j * 128 + 8 * p + 2 * wm) * 2 + wn) * 1024u;
      st16_sc1<0>(z0, wsr, lane * 16u, zso);
      st16_sc1<2048>(z1f, wsr, lane * 16u, zso);
    }
    STAMP(6);
    wait_vm<2>();
    if (grp_arrive_last(L, L_CTR + 44, 8u)) {
      if (lane == 0) st4_sc1<0>(tag, wsr, OFF_FY + (unsigned)(j * 16 + p) * 32u, 0);
    }
    auto z1_loop = [&](auto&& hook) __attribute__((always_inline)) {
      scs = f32x4{0.f, 0.f, 0.f, 0.f};
#pragma unroll
      for (int r = 0; r < 4; ++r) { boff[r] -= L_W2; PIN(boff[r]); }
      static_for<0, 32>([&](auto tc) __attribute__((always_inline)) {
        constexpr int t = decltype(tc)::value, i = t >> 2, r = t & 3, c = i - 1;
        SCHED();
        if constexpr (i >= 1) {
          a1 = mfma32(wb[c & 1][r], xa[c][r], a1);
          if constexpr (r == 0) scs = mfma16(sel, wb[c & 1][0], scs);
        }
        SCHED();
        if constexpr (i < 7) wb[i & 1][r] = tr_frag(L, boff[r] + w1_base(i), boff[r] + w1_base(i) + 512);
        SCHED();
        hook(tc);
      });
      if (lane < 16) {
        *(LAS float*)(L + L_CS1 + colw) = scs[0];
        *(LAS float*)(L + L_CS1 + colw + 256) = scs[1];
      }
      bar();
      STAMP(8);
      const f32x2 c2 = *(LAS f32x2*)(L + L_CS1 + colr);
      a1 = __builtin_amdgcn_mfma_f32_32x32x2f32(c2[0] + c2[1], nmu, a1, 0, 0, 0);
    };
    auto publish_z2n = [&]() __attribute__((always_inline)) {
      if (grp_arrive_last(L, L_CTR + 16, 8u)) {
        if (lane == 0) st4_sc1<0>(tag, wsr, OFF_FA + (unsigned)(j * 16 + p) * 32u, 0);
      }
      STAMP(7);
    };
    if (w >= 4) {
      z1_loop([&](auto tc) __attribute__((always_inline)) {
        constexpr int t = decltype(tc)::value;
        if constexpr (t == FLAGT) { wait_vm<0>(); publish_z2n(); }
        if constexpr (t == YLT) { poll_flags<1>(wsr, fft_flags, true, tag, 3u); load_fft_in(); }
      });
      FFT_IN_LANDED();
    }
    int w_opaque = w;
    asm volatile("" : "+s"(w_opaque));
    if (w_opaque < 4) {
#define ROTMASK 0
      const unsigned rot = (j + 2 * p) & ROTMASK;
      const v4u spr = make_rsrc(P.wsp, 2048u * 4096u);
      const unsigned spv0 = (unsigned)(128 * p + (lane >> 3)) * 4096u + (((lane & 7) ^ (lane >> 4)) << 4);
      const unsigned spv1 = (spv0 ^ 64u) + 8u * 4096u - 1024u, spv2 = spv0 + 16u * 4096u - 2048u, spv3 = (spv0 ^ 64u) + 24u * 4096u - 3072u;
      auto sp_soff = [&](int u) __attribute__((always_inline)) { return (unsigned)w * 1024u + (((u >> 2) ^ rot) * 128) + (unsigned)((u + 1 + w) & 3) * (32 * 4096); };
      auto issue_sp = [&](int u) __attribute__((always_inline)) { dma16x4(spr, spv0, spv1, spv2, spv3, sp_soff(u), lbase + ring_base(u % 7) + w * 4096); };
      z1_loop([&](auto tc) __attribute__((always_inline)) {
        constexpr int t = decltype(tc)::value, q = t - 4;
        if constexpr (q >= 0 && q < 12) {
          if constexpr ((q & 3) == 0) set_m0(lbase + ring_base(q >> 2) + w * 4096);
          dma_piece<(q & 3) * 1024>(spr, (q & 3) == 0 ? spv0 : (q & 3) == 1 ? spv1 : (q & 3) == 2 ? spv2 : spv3, sp_soff(q >> 2));
        }
        if constexpr (t == FLAGT) { wait_vm<12>(); publish_z2n(); }
      });
      __builtin_amdgcn_s_setprio(GPRIO_);
      unsigned rdl[4], rdh[4];
#pragma unroll
      for (int ks = 0; ks < 4; ++ks) {
        rdl[ks] = w * 4096 + r32 * 128 + (((2 * ks + h2) ^ ((r32 >> 1) & 7)) << 4);
        rdh[ks] = rdl[ks] + RING_HI;
        PIN(rdl[ks]);
        PIN(rdh[ks]);
      }
      poll_flags<0>(wsr, OFF_FA + (unsigned)(j * 4 + w) * 128u + (lane & 3) * 32u, true, tag, 2u);
      STAMP(10);
      const unsigned zso = OFF_Z2N + (unsigned)(j * 128 + 32 * w) * 2048u;
      const unsigned zv = lane * 16u;
      v4u bz[2][8];
      auto issue_b = [&](int c) __attribute__((always_inline)) {
        v4u(&d)[8] = bz[c & 1];
        const unsigned so = zso + (c ^ rot) * 8192;
        ld16_sc1<0>(d[0], wsr, zv, so); ld16_sc1<1024>(d[1], wsr, zv, so); ld16_sc1<2048>(d[2], wsr, zv, so); ld16_sc1<3072>(d[3], wsr, zv, so);
        ld16_sc1<0>(d[4], wsr, zv, so + 4096); ld16_sc1<1024>(d[5], wsr, zv, so + 4096); ld16_sc1<2048>(d[6], wsr, zv, so + 4096); ld16_sc1<3072>(d[7], wsr, zv, so + 4096);
      };
      issue_b(0);
      issue_b(1);
      issue_sp(3);
      finish_z1([&](auto hc) __attribute__((always_inline)) { issue_sp(4 + decltype(hc)::value); });
      issue_sp(6);
      STAMP(9);
      f32x16 acc[2][4];
      {
        const float bias = bf_lo(*(LAS unsigned short*)(L + L_BSP + (32 * w + r32) * 2));
        acc[0][3] = splat16(bias);
        acc[1][3] = splat16(bias);
#pragma unroll
        for (int b = 0; b < 3; ++b) { acc[0][b] = splat16(0.f); acc[1][b] = splat16(0.f); }
      }
      v4u af[2][4];
      unsigned dmp;
      static_for<0, 33>([&](auto sc) __attribute__((always_inline)) {
        constexpr int s = decltype(sc)::value;
        constexpr int u = s - 1, c = u >> 2, rg = u & 3;
        v4u(&d)[8] = bz[c & 1];
        v4u(&dn)[8] = bz[(c + 1) & 1];
        if constexpr (s >= 1) asm volatile("" : "+v"(af[u & 1][0]), "+v"(af[u & 1][1]), "+v"(af[u & 1][2]), "+v"(af[u & 1][3])::"memory");
        if constexpr (s < 32) {
          constexpr int need = p2_pos(s) - p2_dma_done(s);
          asm volatile("s_waitcnt vmcnt(%0)" ::"i"(need > 63 ? 63 : need) : "memory");
          constexpr unsigned rb = ring_base(s % 7);
#pragma unroll
          for (int ks = 0; ks < 4; ++ks) af[s & 1][ks] = *(LAS v4u*)(L + (rb >= RING_HI ? rdh[ks] + (rb - RING_HI) : rdl[ks] + rb));
        }
        if constexpr (s == 28) dmp = w * 4096 + fresh_lane() * 16;
        if constexpr (s == A3S || s == 32) grp_arrive(L, L_CTR + 24 + 4 * w);
        SCHED();
        if constexpr (s >= 1) {
          if constexpr (rg == 0) {
            constexpr int need = p2_pos(s) - p2_b_done(c);
            asm volatile("s_waitcnt vmcnt(%8)"
                         : "+v"(d[0]), "+v"(d[1]), "+v"(d[2]), "+v"(d[3]), "+v"(d[4]), "+v"(d[5]), "+v"(d[6]), "+v"(d[7])
                         : "i"(need > 63 ? 63 : need)
                         : "memory");
          }
          const unsigned so = sp_soff(u + 7);
          const unsigned bo = zso + ((c + 1) ^ rot) * 8192 + rg * 4096;
          if constexpr (p2_dma_in(s)) set_m0(lbase + ring_base(u % 7) + w * 4096);
          static_for<0, 4>([&](auto kc) __attribute__((always_inline)) {
            constexpr int ks = decltype(kc)::value;
            acc[0][rg] = mfma32(d[2 * ks], af[u & 1][ks], acc[0][rg]);
            SCHED();
            if constexpr (p2_dma_in(s)) dma_piece<ks * 1024>(spr, ks == 0 ? spv0 : ks == 1 ? spv1 : ks == 2 ? spv2 : spv3, so);
            SCHED();
            acc[1][rg] = mfma32(d[2 * ks + 1], af[u & 1][ks], acc[1][rg]);
            SCHED();
            if constexpr (p2_b_in(s)) ld16_sc1<ks * 1024>(dn[4 * rg + ks], wsr, zv, bo);
            if constexpr (s >= 30) {
              const f32x16& t = acc[ks >> 1][rg - 1];
              constexpr int o = 8 * (ks & 1);
              *(LAS v4u*)(L + dmp + ring_base(3 + rg) + ks * 1024) =
                  v4u{pk_bf(t[o], t[o + 1]), pk_bf(t[o + 2], t[o + 3]), pk_bf(t[o + 4], t[o + 5]), pk_bf(t[o + 6], t[o + 7])};
            }
            SCHED();
          });
        }
      });
      __builtin_amdgcn_s_setprio(0);
      STAMP(11);
      RELANE();
      v4u es[2];
#pragma unroll
      for (int s2 = 0; s2 < 2; ++s2)
#pragma unroll
        for (int e = 0; e < 4; ++e) {
          const unsigned k0 = 16 * s2 + 8 * (e >> 1) + 4 * h2 + 2 * (e & 1);
          es[s2][e] = r32 == k0 ? 0x00003f80u : r32 == k0 + 1 ? 0x3f800000u : 0u;
        }
      PIN(es[0]); PIN(es[1]);
      grp_sync(L, L_CTR + 0, 8u);
      STAMP(19);
      {
        v4u pt[3][4], z1q[4];
#pragma unroll
        for (int q = 0; q < 4; ++q) {
#pragma unroll
          for (int rel = 1; rel < 4; ++rel) pt[rel - 1][q] = *(LAS v4u*)(L + ring_base(3 + rel) + ((w - rel) & 3) * 4096 + q * 1024 + lane * 16);
          if (q & 1) {
            z1q[q - 1] = *(LAS v4u*)(L + L_Z1 + ((q >> 1) * 4 + w) * 2048 + lane * 16);
            z1q[q] = *(LAS v4u*)(L + L_Z1 + ((q >> 1) * 4 + w) * 2048 + 1024 + lane * 16);
          }
        }
        SCHED();
#pragma unroll
        for (int q = 0; q < 4; ++q)
#pragma unroll
          for (int rel = 0; rel < 3; ++rel) acc[q >> 1][3] = mfma32(es[q & 1], pt[rel][q], acc[q >> 1][3]);
        SCHED();
        grp_arrive(L, L_CTR + 20);
#pragma unroll
        for (int q = 0; q < 4; ++q) {
          const f32x16& t = acc[q >> 1][3];
          v4u hq;
#pragma unroll
          for (int e = 0; e < 4; ++e) hq[e] = pk_bf(bf_lo(z1q[q][e]) * t[8 * (q & 1) + 2 * e], bf_hi(z1q[q][e]) * t[8 * (q & 1) + 2 * e + 1]);
          st16_sc1<0>(hq, wsr, lane * 16u, OFF_H + (unsigned)((p * 4 + w) * 56 + 4 * j + q) * 1024u);
          SCHED();
        }
      }
      STAMP(12);
      wait_vm<0>();
      if (grp_arrive_last(L, L_CTR + 4, 4u)) {
        const int kq = lane ? (4 * j + 3) / 14 : (4 * j) / 14;
        if (lane < 2) st4_sc1<0>(tag, wsr, OFF_FH + (unsigned)(p * 4 + kq) * 128u + (unsigned)(j - (7 * kq) / 2) * 32u, 0);
      }
      STAMP(13);
      asm volatile("" : "=v"(a1), "=v"(gk1), "=v"(gk2), "=v"(gsg), "=v"(wq[0]), "=v"(wq[1]));
      asm volatile("" : "=v"(yin[0]), "=v"(yin[1]), "=v"(yin[2]), "=v"(yin[3]), "=v"(yin[4]), "=v"(yin[5]), "=v"(yin[6]), "=v"(yin[7]));
    }
  } else {
    if (tid < 16) *(LAS unsigned*)(L + L_CTR + 4 * tid) = 0u;
    {
      const int rh = sidx >> 4, lb = sidx & 15;
      const unsigned so = (unsigned)(16 * lb + 256 * (4 * rh + (w & 3))) * 450u;
      const unsigned v0 = lane * 16u;
      const v4u rs = make_rsrc(w < 4 ? P.wr : P.wi, 2048u * 450u);
      dma16x4(rs, v0, v0, v0, v0, so, lbase + w * 8192);
      dma16x4(rs, v0, v0, v0, v0, so + 4096, lbase + w * 8192 + 4096);
      unsigned epoch_v;
      asm volatile("buffer_load_dword %0, off, %1, 0 sc1\n\ts_waitcnt vmcnt(0)" : "=v"(epoch_v) : "s"(wsr) : "memory");
      GET_TAG(epoch_v);
      bar();
      const unsigned q = (tid >> 2) & 3, li = tid & 3;
#pragma unroll
      for (int hf = 0; hf < 2; ++hf) {
        unsigned short e[4][8];
        unsigned kk[4];
#pragma unroll
        for (int it = 0; it < 4; ++it) {
          kk[it] = (tid >> 4) + 32 * (4 * hf + it);
          kk[it] = kk[it] < 225 ? kk[it] : 224;
          const unsigned a = (4 * li + q) * 450 + kk[it] * 2;
#pragma unroll
          for (int r = 0; r < 8; ++r) e[it][r] = *(LAS unsigned short*)(L + a + r * 8192);
        }
        SCHED();
#pragma unroll
        for (int it = 0; it < 4; ++it) {
          v4u o;
#pragma unroll
          for (int r = 0; r < 4; ++r) o[r] = (unsigned)e[it][r] | ((unsigned)e[it][4 + r] << 16);
          st16_sc1<0>(o, wsr, (((kk[it] * 4 + q) * 2 + rh) * 64 + 4 * lb + li) * 16u, OFF_WT);
        }
      }
      wait_vm<0>();
      bar();
      if (tid == 0) st4_sc1<0>(tag, wsr, OFF_FW + (unsigned)sidx * 32u, 0);
      STAMP(8);
    }
#define TOUCH 1
    if (sidx != 0 || w < 4) {
      unsigned dmy = 0;
      if (TOUCH & 1) {
        const v4u spr = make_rsrc(P.wsp, 2048u * 4096u);
        const unsigned vo = lane * 4096u, so = (unsigned)(128 * p + 64 * (w >> 2)) * 4096u + (unsigned)(w & 3) * 1024u;
#pragma unroll
        for (int i = 0; i < 8; ++i)
          asm volatile("buffer_load_dword %0, %1, %2, %3 offen offset:%4" : "+v"(dmy) : "v"(vo), "s"(spr), "s"(so), "i"(i * 128) : "memory");
      }
      STAMP(9);
      if (TOUCH & 2) {
        if (w < 7) {
          const int jz = 7 * (xcd & 1) + w, kz = rr & 3;
          poll_flags<16>(wsr, OFF_FA + (unsigned)(jz * 16 + 4 * kz + 2 + (lane & 1)) * 32u, true, tag, 7u);
          const unsigned so = OFF_Z2N + (unsigned)(jz * 128 + 32 * kz + 16) * 2048u;
#pragma unroll
          for (int i = 0; i < 4; ++i)
            asm volatile("buffer_load_dword %0, %1, %2, %3 offen sc1" : "+v"(dmy) : "v"(lane * 128u), "s"(wsr), "s"(so + i * 8192u) : "memory");
        }
      }
      if (TOUCH & 1) {
        const v4u wor = make_rsrc(P.wout, 896u * 896u);
#pragma unroll
        for (int i = 0; i < 3; ++i)
          asm volatile("buffer_load_dword %0, %1, %2, %3 offen" : "+v"(dmy) : "v"(lane * 128u), "s"(wor), "s"((unsigned)(((rr & 3) * 8 + w) * 3 + i) * 8192u) : "memory");
      }
      asm volatile("s_waitcnt vmcnt(0)" : "+v"(dmy)::"memory");
      STAMP(10);
    }
    if (w < 4) {
      STAMP_OUT();
      return;
    }
    if (sidx != 0) {
      STAMP_OUT();
      return;
    }
    poll_flags<8>(wsr, fft_flags, true, tag, 3u);
    load_fft_in();
    FFT_IN_LANDED();
  }

  if (w >= 4) {
#define FPRIO 2
#define FPRIO_C 0
#define FPRIO_G 0
    __builtin_amdgcn_s_setprio(FPRIO);
    const int fw = w - 4;
    const unsigned tp = tid - 256;
    const int mode = is_main ? (slot == 0 ? 1 : 0) : 2;
    const unsigned myreg = L_FFT + 4096u * fw;
    cf t1[8], tb[7], tc[7];
    FftAddr FA;
    {
      const float a1 = (float)(fw * lane) * (1.0f / 2048.0f), d1 = (float)fw * (1.0f / 32.0f);
      const float ab = (float)(lane & 7) * (1.0f / 64.0f), ac = (float)lane * (1.0f / 512.0f);
#pragma unroll
      for (int r = 0; r < 8; ++r) {
        const float a = __builtin_fmaf((float)r, d1, a1);
        t1[r] = cf{__builtin_amdgcn_cosf(a) * 0.022097086912079612f, __builtin_amdgcn_sinf(a) * 0.022097086912079612f};
        PIN(t1[r].x); PIN(t1[r].y);
        FA.wa[r] = myreg + 8 * fsw(8 * lane + r);
        FA.wb[r] = myreg + 8 * fsw((lane >> 3) * 64 + (lane & 7) + 8 * r);
        PIN(FA.wa[r]); PIN(FA.wb[r]);
      }
#pragma unroll
      for (int r = 1; r < 8; ++r) {
        const float a = r * ab, b = r * ac;
        tb[r - 1] = cf{__builtin_amdgcn_cosf(a), __builtin_amdgcn_sinf(a)};
        tc[r - 1] = cf{__builtin_amdgcn_cosf(b), __builtin_amdgcn_sinf(b)};
        PIN(tb[r - 1].x); PIN(tb[r - 1].y); PIN(tc[r - 1].x); PIN(tc[r - 1].y);
      }
      FA.rd[0] = myreg + 8 * fsw(lane);
      FA.rd[1] = myreg + 8 * fsw(lane + 64) - 512;
      PIN(FA.rd[0]); PIN(FA.rd[1]);
    }
    unsigned e1 = L_FFT + 8 * fsw(tp), e2a = L_FFT + 8 * fsw(2 * tp), e2b = e2a ^ 8u;
    PIN(e1); PIN(e2a); PIN(e2b);
    STAMP(10);
    __builtin_amdgcn_s_setprio(FPRIO_C);
    if (mode != 0) {
#pragma unroll
      for (int m = 0; m < 8; ++m) yin[m] = mode == 2 ? yin[m] >> 16 : yin[m] & 0xffffu;
    }
    cf v[8];
#pragma unroll
    for (int m = 0; m < 8; ++m) {
      const h16x2 hv = __builtin_bit_cast(h16x2, yin[m]);
      v[m] = cf{(float)hv[0], (float)hv[1]};
    }
    dft4<-1>(v[0], v[2], v[4], v[6]);
    dft4<-1>(v[1], v[3], v[5], v[7]);
#pragma unroll
    for (int q = 0; q < 4; ++q) {
      lds_wc(L, e1 + 4096 * q, v[2 * q]);
      lds_wc(L, e1 + 4096 * q + 2048, v[2 * q + 1]);
    }
    grp_sync(L, L_CTR + 8, 4u);
#pragma unroll
    for (int r = 0; r < 8; ++r) v[r] = ctw<-1>(lds_rc(L, FA.rd[r & 1] + 512 * r), t1[r]);
    FENCE();
    fft512<-1>(v, tb, tc, L, FA);
#pragma unroll
    for (int r = 0; r < 8; ++r) {
      const unsigned u = wq[r >> 2][r & 3];
      v[r] = ctw<1>(v[r], cf{bf_lo(u), bf_hi(u)});
    }
    fft512<1>(v, tb, tc, L, FA);
#pragma unroll
    for (int r = 0; r < 8; ++r) lds_wc(L, FA.rd[r & 1] + 512 * r, ctw<1>(v[r], t1[r]));
    grp_sync(L, L_CTR + 8, 8u);
#pragma unroll
    for (int q = 0; q < 4; ++q) {
      v[2 * q] = lds_rc(L, e2a + 4096 * q);
      v[2 * q + 1] = lds_rc(L, e2b + 4096 * q);
    }
    dft4<1>(v[0], v[2], v[4], v[6]);
    dft4<1>(v[1], v[3], v[5], v[7]);
    __builtin_amdgcn_s_setprio(FPRIO);
    {
      const unsigned so = OFF_ZT + (unsigned)slot * 8192u;
      if (mode == 0) {
        st8_sc1<0>(v2u{pk_h(v[0].x, v[0].y), pk_h(v[1].x, v[1].y)}, wsr, tp * 8u, so);
        st8_sc1<2048>(v2u{pk_h(v[2].x, v[2].y), pk_h(v[3].x, v[3].y)}, wsr, tp * 8u, so);
        st8_sc1<0>(v2u{pk_h(v[4].x, v[4].y), pk_h(v[5].x, v[5].y)}, wsr, tp * 8u, so + 4096);
        st8_sc1<2048>(v2u{pk_h(v[6].x, v[6].y), pk_h(v[7].x, v[7].y)}, wsr, tp * 8u, so + 4096);
      } else {
        const unsigned vo = tp * 8u + (mode == 2 ? 2u : 0u);
        st2_sc1<0>(pk_h(v[0].x, 0.f), wsr, vo, so); st2_sc1<4>(pk_h(v[1].x, 0.f), wsr, vo, so);
        st2_sc1<2048>(pk_h(v[2].x, 0.f), wsr, vo, so); st2_sc1<2052>(pk_h(v[3].x, 0.f), wsr, vo, so);
        st2_sc1<0>(pk_h(v[4].x, 0.f), wsr, vo, so + 4096); st2_sc1<4>(pk_h(v[5].x, 0.f), wsr, vo, so + 4096);
        st2_sc1<2048>(pk_h(v[6].x, 0.f), wsr, vo, so + 4096); st2_sc1<2052>(pk_h(v[7].x, 0.f), wsr, vo, so + 4096);
      }
    }
    STAMP(11);
    const unsigned g4 = lane >> 4, n16 = lane & 15;
    v4u gf[4][4];
    auto make_g = [&](int i) __attribute__((always_inline)) {
      int u = 4 * i + fw;
      u = u < 14 ? u : 13;
      const int ks = u < 2 ? u + 12 : u - 2;
#pragma unroll
      for (int d4 = 0; d4 < 4; ++d4) {
        const unsigned dcol = 64 * dt + 16 * ((d4 + fw) & 3) + n16;
        const float df = (float)dcol, fa = rev448(df, (float)(16 * ks + 4 * g4)), fs = df * (1.0f / 448.0f);
#pragma unroll
        for (int e = 0; e < 4; ++e) {
          const float a = __builtin_fmaf((float)e, fs, fa);
          gf[i][d4][e] = pk_h(__builtin_amdgcn_cosf(a), __builtin_amdgcn_sinf(-a));
        }
        if (i == 0) {
          if (fw == 2 && g4 == 0) gf[0][d4][0] = (n16 & 1) ? 0xb8003800u : 0x38003800u;
        }
        PIN(gf[i][d4]);
      }
    };
    __builtin_amdgcn_s_setprio(FPRIO_G);
    if (is_main) {
      finish_z1([](auto) {});
      grp_arrive(L, L_CTR + 0);
    }
    __builtin_amdgcn_s_setprio(FPRIO);
    wait_vm<0>();
    if (grp_arrive_last(L, L_CTR + 12, 4u)) {
      if (lane == 0) st4_sc1<0>(tag, wsr, OFF_FZ + (unsigned)kw * 16u, 0);
    }
    STAMP(12);
    if (!is_main) {
      STAMP_OUT();
      return;
    }
    __builtin_amdgcn_s_setprio(FPRIO_G);
    make_g(0);
    make_g(1);
    make_g(2);
    make_g(3);
    __builtin_amdgcn_s_setprio(FPRIO);
    grp_sync(L, L_CTR + 8, 12u);
    {
      const unsigned ug = 4 * g4 + fw;
      const unsigned kg = ug < 2 ? ug + 12 : ug - 2;
      const unsigned fo = ug < 14 ? kg * 256u + n16 * 16u : 224u * 16u;
      poll_flags<6>(wsr, OFF_FZ + fo, ug <= 14, tag, 5u);
    }
    STAMP(13);
    __builtin_amdgcn_s_setprio(0);
    grp_wait_sleep(L, L_CTR + 24 + 4 * fw, 1u);
    __builtin_amdgcn_s_setprio(FPRIO);
    STAMP(19);
    v4u zr[4][4];
    {
      const unsigned vo = g4 * 32768u + n16 * 16u;
#pragma unroll
      for (int i = 0; i < 4; ++i) {
        int u = 4 * i + fw;
        u = u < 14 ? u : 13;
        const int ks = u < 2 ? u + 12 : u - 2;
        const unsigned so = OFF_ZT + (unsigned)ks * (16u * 8192u) + (unsigned)(128 * p + 64 * th) * 4u;
#pragma unroll
        for (int c = 0; c < 4; ++c) ld16_sc1<0>(zr[i][c], wsr, (i == 3 && fw >= 2) ? WS_BYTES : vo, so + c * 8192);
      }
      const unsigned rv = (unsigned)(128 * p + 64 * th + 16 * fw + (lane >> 3)) * 896u + (unsigned)dt * 128u + (lane & 7) * 16u;
      dma16x2(xr, rv, rv + 8u * 896u - 1024u, 0, lbase + L_XR + fw * 2048);
    }
    __builtin_amdgcn_s_setprio(FPRIO_G);
    f32x4 sacc[4][4];
#pragma unroll
    for (int a = 0; a < 4; ++a)
#pragma unroll
      for (int b = 0; b < 4; ++b) sacc[a][b] = f32x4{0.f, 0.f, 0.f, 0.f};
#pragma unroll
    for (int i = 0; i < 4; ++i) {
      asm volatile("s_waitcnt vmcnt(%4)" : "+v"(zr[i][0]), "+v"(zr[i][1]), "+v"(zr[i][2]), "+v"(zr[i][3]) : "i"(14 - 4 * i) : "memory");
      if (i < 3 || fw < 2) {
#pragma unroll
        for (int ti = 0; ti < 4; ++ti) {
          const v4u zf = v4u{zr[i][0][ti], zr[i][1][ti], zr[i][2][ti], zr[i][3][ti]};
#pragma unroll
          for (int d4 = 0; d4 < 4; ++d4) sacc[ti][d4] = mfma_h(gf[i][d4], zf, sacc[ti][d4]);
        }
      }
      SCHED();
    }
    const v4u wor = make_rsrc(P.wout, 896u * 896u);
    const unsigned ov0 = (lane >> 3) * 896u + (unsigned)dt * 128u + (((lane & 7) ^ (((lane >> 4) & 1) << 2)) << 4);
    const unsigned ov1 = ov0 + 8u * 896u - 1024u, ov2 = ov0 + 16u * 896u - 2048u, ov3 = ov0 + 24u * 896u - 3072u;
    auto issue_wo = [&](int i) __attribute__((always_inline)) { dma16x4(wor, ov0, ov1, ov2, ov3, (unsigned)((7 * fw + i) * 32) * 896u, lbase + ring_base(i) + fw * 4096); };
#pragma unroll
    for (int r = 0; r < 4; ++r) {
      const int q = (fw + r) & 3;
      const unsigned sb = L_FFT + 4096u * q + lane * 16;
      if (r == 1) {
        grp_wait(L, L_CTR + 24 + 4 * fw, 2u);
#pragma unroll
        for (int i = 0; i < 4; ++i) issue_wo(i);
      }
      if (r == 2) {
        grp_wait(L, L_CTR + 20, 4u);
#pragma unroll
        for (int i = 4; i < 7; ++i) issue_wo(i);
      }
      if (r > 0) {
        grp_sync(L, L_CTR + 8, 12u + 4u * r);
#pragma unroll
        for (int ti = 0; ti < 4; ++ti) sacc[ti][r] += *(LAS f32x4*)(L + sb + ti * 1024);
      }
#pragma unroll
      for (int ti = 0; ti < 4; ++ti) *(LAS f32x4*)(L + sb + ti * 1024) = sacc[ti][r];
    }
    __builtin_amdgcn_s_setprio(0);
    STAMPX(1, 9);
    wait_vm<0>();
  }

  RELANE();
  const int tp2 = w & 1, kq = w >> 1;
  unsigned ob[2][2];
#pragma unroll
  for (int d2 = 0; d2 < 2; ++d2) {
    ob[d2][0] = kq * 4096 + (4 * h2 + trq) * 128 + ((d2 ^ (trq >> 1)) << 6) + 32 * trb + 8 * trp;
    ob[d2][1] = ob[d2][0] + RING_HI;
    PIN(ob[d2][0]); PIN(ob[d2][1]);
  }
  bar();
  STAMP(15);
  f32x16 oa[2] = {splat16(0.f), splat16(0.f)};
  {
    v4u wo[14][2];
#pragma unroll
    for (int i = 0; i < 14; ++i)
#pragma unroll
      for (int d2 = 0; d2 < 2; ++d2) {
        const unsigned rb = ring_base(i >> 1);
        const unsigned a0 = (rb >= RING_HI ? ob[d2][1] + (rb - RING_HI) : ob[d2][0] + rb) + (i & 1) * 2048;
        wo[i][d2] = tr_frag(L, a0, a0 + 1024);
      }
    SCHED();
    poll_flags<0>(wsr, OFF_FH + (unsigned)(p * 4 + kq) * 128u + (lane & 3) * 32u, true, tag, 6u);
    STAMP(16);
    v4u hf[14];
    const unsigned so = OFF_H + (unsigned)((p * 4 + 2 * th + tp2) * 56 + 14 * kq) * 1024u;
    static_for<0, 14>([&](auto ic) __attribute__((always_inline)) {
      constexpr int i = decltype(ic)::value;
      ld16_sc1<(i & 3) * 1024>(hf[i], wsr, lane * 16u, so + (i >> 2) * 4096);
    });
    grp_arrive(L, L_CTR + 40);
    SCHED();
    static_for<0, 14>([&](auto ic) __attribute__((always_inline)) {
      constexpr int i = decltype(ic)::value;
      asm volatile("s_waitcnt vmcnt(%1)" : "+v"(hf[i]) : "i"(13 - i) : "memory");
      oa[0] = mfma32(wo[i][0], hf[i], oa[0]);
      oa[1] = mfma32(wo[i][1], hf[i], oa[1]);
      SCHED();
      if constexpr (i == 11) grp_wait(L, L_CTR + 40, 8u);
      SCHED();
    });
  }
  STAMP(17);
  {
    const unsigned rb = 16384u * (2 * tp2 + (r32 >> 4)) + kq * 4096 + (r32 & 15) * 256;
#pragma unroll
    for (int d2 = 0; d2 < 2; ++d2)
#pragma unroll
      for (int g = 0; g < 4; ++g)
        *(LAS f32x4*)(L + rb + d2 * 128 + (((2 * g + h2) ^ (r32 & 7)) << 4)) = f32x4{oa[d2][4 * g], oa[d2][4 * g + 1], oa[d2][4 * g + 2], oa[d2][4 * g + 3]};
  }
  {
    const unsigned t = tid >> 3, c8 = tid & 7, tr = t & 15;
    const unsigned sb = L_FFT + (c8 >> 1) * 4096 + (t & 3) * 1024 + (c8 & 1) * 512 + (t >> 2) * 16;
    const f32x4 s0 = *(LAS f32x4*)(L + sb), s1 = *(LAS f32x4*)(L + sb + 256);
    const v4u xq = *(LAS v4u*)(L + L_XR + tid * 16);
    unsigned c0 = 16384u * (w >> 1) + tr * 256 + (((2 * c8) ^ (tr & 7)) << 4), c1 = c0 ^ 16u;
    unsigned oo = (128 * p + 64 * th + t) * 896 + dt * 128 + c8 * 16;
    PIN(c0); PIN(c1); PIN(oo);
    bar();
    f32x4 o0[4], o1[4];
#pragma unroll
    for (int q = 0; q < 4; ++q) {
      o0[q] = *(LAS f32x4*)(L + q * 4096 + c0);
      o1[q] = *(LAS f32x4*)(L + q * 4096 + c1);
    }
    SCHED();
    float o[8], u[8], r[8];
#pragma unroll
    for (int e = 0; e < 4; ++e) {
      o[e] = (o0[0][e] + o0[1][e]) + (o0[2][e] + o0[3][e]);
      o[4 + e] = (o1[0][e] + o1[1][e]) + (o1[2][e] + o1[3][e]);
    }
#pragma unroll
    for (int e = 0; e < 8; ++e) u[e] = o[e] * -1.442695041f;
    SCHED();
#pragma unroll
    for (int e = 0; e < 8; ++e) u[e] = __builtin_amdgcn_exp2f(u[e]);
#pragma unroll
    for (int e = 0; e < 8; ++e) o[e] *= e < 4 ? s0[e & 3] : s1[e & 3];
    SCHED();
#pragma unroll
    for (int e = 0; e < 8; ++e) u[e] = __builtin_fmaf(u[e], 224.0f, 224.0f);
    SCHED();
#pragma unroll
    for (int e = 0; e < 8; ++e) u[e] = __builtin_amdgcn_rcpf(u[e]);
#pragma unroll
    for (int e = 0; e < 8; ++e) r[e] = (e & 1) ? bf_hi(xq[e >> 1]) : bf_lo(xq[e >> 1]);
    SCHED();
#pragma unroll
    for (int e = 0; e < 8; ++e) r[e] = __builtin_fmaf(o[e], u[e], r[e]);
    const v4u res = v4u{pk_bf(r[0], r[1]), pk_bf(r[2], r[3]), pk_bf(r[4], r[5]), pk_bf(r[6], r[7])};
    *(GAS v4u*)((GAS unsigned char*)P.out + oo) = res;
  }
  if (bid == 0 && tid == 0) st4_sc1<0>(tag, wsr, OFF_EPOCH, 0);
  STAMP(18);
  STAMP_OUT();
}
__device__ __attribute__((used, noinline)) void code_end_marker() { asm volatile("code_end_lbl:"); }

extern "C" void kernel_launch(void* const* d_in, const int* in_sizes, int n_in, void* d_out, int out_size, void* d_ws, size_t ws_size,
                              hipStream_t stream) {
  Params P;
  P.x = (const unsigned short*)d_in[0];
  P.wr = (const unsigned short*)d_in[1];
  P.wi = (const unsigned short*)d_in[2];
  P.win = (const unsigned short*)d_in[4];
  P.wsp = (const unsigned short*)d_in[5];
  P.bsp = (const unsigned short*)d_in[6];
  P.wout = (const unsigned short*)d_in[7];
  P.out = (unsigned short*)d_out;
  P.ws = (unsigned char*)d_ws;
  fused_kernel<<<256, 512, 0, stream>>>(P);
}
```

```cpp
#define LATE0 4
#include <hip/hip_runtime.h>
#include <stdint.h>

typedef unsigned v4u __attribute__((ext_vector_type(4)));
typedef unsigned v2u __attribute__((ext_vector_type(2)));
typedef float f32x4 __attribute__((ext_vector_type(4)));
typedef float f32x2 __attribute__((ext_vector_type(2)));
typedef float f32x16 __attribute__((ext_vector_type(16)));
typedef short s16x8 __attribute__((ext_vector_type(8)));
typedef short s16x4 __attribute__((ext_vector_type(4)));
typedef _Float16 h16x8 __attribute__((ext_vector_type(8)));
typedef _Float16 h16x2 __attribute__((ext_vector_type(2)));
typedef __bf16 b16x2 __attribute__((ext_vector_type(2)));
#define LAS __attribute__((address_space(3)))
#define GAS __attribute__((address_space(1)))
#define CAS __attribute__((address_space(4)))
#define DEVINL __device__ __forceinline__
#if defined(__HIP_DEVICE_COMPILE__)
#define NO_PK __attribute__((target("no-packed-fp32-ops")))
#else
#define NO_PK
#endif

#define STAMPS 0

constexpr unsigned OFF_EPOCH = 0, OFF_DBG = 128;
constexpr unsigned OFF_FA = 4096;
constexpr unsigned OFF_FY = 12288;
constexpr unsigned OFF_FW = 20480;
constexpr unsigned OFF_FZ = 24576;
constexpr unsigned OFF_FH = 32768;
constexpr unsigned OFF_ST = 65536;
constexpr unsigned OFF_Z2N = 1u << 20;
constexpr unsigned OFF_YT = 5u << 20;
constexpr unsigned OFF_WT = 7u << 20;
constexpr unsigned OFF_ZT = 9u << 20;
constexpr unsigned OFF_H = 11u << 20;
constexpr unsigned OFF_STAMP = 16u << 20;
constexpr unsigned WS_BYTES = 20u << 20;

constexpr unsigned L_F = 65536;
constexpr unsigned L_W2 = 122880;
constexpr unsigned L_FFT = 114688;
constexpr unsigned L_Z1 = 131072;
constexpr unsigned L_XR = 147456;
constexpr unsigned L_CS2 = 155648;
constexpr unsigned L_CS1 = L_CS2 + 1024;
constexpr unsigned L_XS = L_CS1 + 1024;
constexpr unsigned L_CTR = L_XS + 1024;
constexpr unsigned L_BSP = L_CTR + 256;
constexpr unsigned L_ZS = L_CTR + 1024;
constexpr unsigned L_PS = L_ZS + 1024;
constexpr unsigned LDS_BYTES = 163840;
__host__ __device__ __forceinline__ constexpr unsigned w1_base(int i) { return i < 2 ? 49152u + 8192u * i : 8192u * (i - 2); }
__host__ __device__ __forceinline__ constexpr unsigned ring_base(int i) { return i < 3 ? 65536u + 16384u * i : 16384u * (i - 3); }
constexpr unsigned RING_HI = 65536;

#define GPRIO_ 1
#define GATH 6
#define STWARM 1
#define FLAGT 22
#define YLT 31
#define A3S 26
#define WOG 0
#define WOR 0
#define ZLATE 1
#define HSL 2
#define ASL 0
constexpr unsigned SPIN_CAP = 1u << 14;

struct Params {
  const unsigned short* x;
  const unsigned short* wr;
  const unsigned short* wi;
  const unsigned short* win;
  const unsigned short* wsp;
  const unsigned short* bsp;
  const unsigned short* wout;
  unsigned short* out;
  unsigned char* ws;
};

template <int V> struct IC { static constexpr int value = V; };
template <int I, int N, class F> DEVINL void static_for(F&& f) {
  if constexpr (I < N) {
    f(IC<I>{});
    static_for<I + 1, N>(f);
  }
}
DEVINL v4u make_rsrc(const void* p, unsigned bytes) {
  const unsigned long long a = (unsigned long long)p;
  v4u r;
  r[0] = __builtin_amdgcn_readfirstlane((unsigned)a);
  r[1] = __builtin_amdgcn_readfirstlane((unsigned)(a >> 32) & 0xffffu);
  r[2] = bytes;
  r[3] = 0x00020000u;
  return r;
}
DEVINL void dma16(v4u rs, unsigned voff, unsigned soff, unsigned lds) {
  asm volatile("s_mov_b32 m0, %3\n\ts_nop 0\n\tbuffer_load_dwordx4 %0, %1, %2 offen lds" ::"v"(voff), "s"(rs), "s"(soff), "s"(lds) : "memory");
}
DEVINL void dma16x2(v4u rs, unsigned v0, unsigned v1, unsigned soff, unsigned lds) {
  asm volatile("s_mov_b32 m0, %4\n\ts_nop 0\n\tbuffer_load_dwordx4 %0, %2, %3 offen lds\n\tbuffer_load_dwordx4 %1, %2, %3 offen offset:1024 lds" ::"v"(v0),
               "v"(v1), "s"(rs), "s"(soff), "s"(lds)
               : "memory");
}
DEVINL void dma16x4(v4u rs, unsigned v0, unsigned v1, unsigned v2, unsigned v3, unsigned soff, unsigned lds) {
  asm volatile(
      "s_mov_b32 m0, %6\n\ts_nop 0\n\tbuffer_load_dwordx4 %0, %4, %5 offen lds\n\tbuffer_load_dwordx4 %1, %4, %5 offen offset:1024 lds\n\t"
      "buffer_load_dwordx4 %2, %4, %5 offen offset:2048 lds\n\tbuffer_load_dwordx4 %3, %4, %5 offen offset:3072 lds" ::"v"(v0),
      "v"(v1), "v"(v2), "v"(v3), "s"(rs), "s"(soff), "s"(lds)
      : "memory");
}
DEVINL void set_m0(unsigned lds) { asm volatile("s_mov_b32 m0, %0\n\ts_nop 0" ::"s"(lds) : "memory"); }
template <int IMM> DEVINL void dma_piece(v4u rs, unsigned voff, unsigned soff) {
  asm volatile("buffer_load_dwordx4 %0, %1, %2 offen offset:%3 lds" ::"v"(voff), "s"(rs), "s"(soff), "i"(IMM) : "memory");
}
DEVINL void dma4(v4u rs, unsigned voff, unsigned soff, unsigned lds) {
  asm volatile("s_mov_b32 m0, %3\n\ts_nop 0\n\tbuffer_load_dword %0, %1, %2 offen lds" ::"v"(voff), "s"(rs), "s"(soff), "s"(lds) : "memory");
}
template <int IMM> DEVINL void ld16_sc1(v4u& d, v4u rs, unsigned voff, unsigned soff) {
  asm volatile("buffer_load_dwordx4 %0, %1, %2, %3 offen offset:%4 sc1" : "=v"(d) : "v"(voff), "s"(rs), "s"(soff), "i"(IMM) : "memory");
}
template <int IMM> DEVINL void ld4_sc1(unsigned& d, v4u rs, unsigned voff, unsigned soff) {
  asm volatile("buffer_load_dword %0, %1, %2, %3 offen offset:%4 sc1" : "=v"(d) : "v"(voff), "s"(rs), "s"(soff), "i"(IMM) : "memory");
}
template <int IMM> DEVINL void st16_sc1(v4u d, v4u rs, unsigned voff, unsigned soff) {
  asm volatile("buffer_store_dwordx4 %0, %1, %2, %3 offen offset:%4 sc1\n\ts_nop 1" ::"v"(d), "v"(voff), "s"(rs), "s"(soff), "i"(IMM) : "memory");
}
template <int IMM> DEVINL void st8_sc1(v2u d, v4u rs, unsigned voff, unsigned soff) {
  asm volatile("buffer_store_dwordx2 %0, %1, %2, %3 offen offset:%4 sc1" ::"v"(d), "v"(voff), "s"(rs), "s"(soff), "i"(IMM) : "memory");
}
template <int IMM> DEVINL void st4_sc1(unsigned d, v4u rs, unsigned voff, unsigned soff) {
  asm volatile("buffer_store_dword %0, %1, %2, %3 offen offset:%4 sc1" ::"v"(d), "v"(voff), "s"(rs), "s"(soff), "i"(IMM) : "memory");
}
template <int IMM> DEVINL void st2_sc1(unsigned d, v4u rs, unsigned voff, unsigned soff) {
  asm volatile("buffer_store_short %0, %1, %2, %3 offen offset:%4 sc1" ::"v"(d), "v"(voff), "s"(rs), "s"(soff), "i"(IMM) : "memory");
}
template <int N> DEVINL void wait_vm() { asm volatile("s_waitcnt vmcnt(%0)" ::"i"(N) : "memory"); }
DEVINL void bar() {
  asm volatile("s_waitcnt lgkmcnt(0)" ::: "memory");
  __builtin_amdgcn_s_barrier();
  asm volatile("" ::: "memory");
}
#define SCHED() __builtin_amdgcn_sched_barrier(0)
#define PIN(x) asm volatile("" : "+v"(x))
#define FENCE() asm volatile("" ::: "memory")

DEVINL f32x16 mfma32(v4u a, v4u b, f32x16 c) {
  return __builtin_amdgcn_mfma_f32_32x32x16_bf16(__builtin_bit_cast(s16x8, a), __builtin_bit_cast(s16x8, b), c, 0, 0, 0);
}
DEVINL f32x4 mfma16(v4u a, v4u b, f32x4 c) {
  return __builtin_amdgcn_mfma_f32_16x16x32_bf16(__builtin_bit_cast(s16x8, a), __builtin_bit_cast(s16x8, b), c, 0, 0, 0);
}
DEVINL f32x4 mfma_h(v4u a, v4u b, f32x4 c) {
  return __builtin_amdgcn_mfma_f32_16x16x32_f16(__builtin_bit_cast(h16x8, a), __builtin_bit_cast(h16x8, b), c, 0, 0, 0);
}
DEVINL f32x16 splat16(float v) { return f32x16{v, v, v, v, v, v, v, v, v, v, v, v, v, v, v, v}; }
DEVINL unsigned pk_bf(float a, float b) { return __builtin_bit_cast(unsigned, __builtin_convertvector((f32x2{a, b}), b16x2)); }
DEVINL unsigned pk_h(float a, float b) { return __builtin_bit_cast(unsigned, __builtin_convertvector((f32x2{a, b}), h16x2)); }
DEVINL float asf(unsigned u) { return __builtin_bit_cast(float, u); }
DEVINL unsigned asu(float f) { return __builtin_bit_cast(unsigned, f); }
DEVINL float bf_lo(unsigned u) { return __builtin_bit_cast(float, u << 16); }
DEVINL float bf_hi(unsigned u) { return __builtin_bit_cast(float, u & 0xffff0000u); }
template <class H> DEVINL void gelu16(f32x16& t, float k1, float k2, float sg, H&& hook) {
  float u[16];
#pragma unroll
  for (int e = 0; e < 16; ++e) u[e] = t[e] * t[e];
#pragma unroll
  for (int e = 0; e < 16; ++e) u[e] = __builtin_fmaf(u[e], k2, k1);
  SCHED();
#pragma unroll
  for (int e = 0; e < 16; ++e) u[e] = u[e] * t[e];
  SCHED();
  hook(IC<0>{});
  SCHED();
#pragma unroll
  for (int e = 0; e < 16; ++e) u[e] = __builtin_amdgcn_exp2f(u[e]);
  SCHED();
#pragma unroll
  for (int e = 0; e < 16; ++e) u[e] = __builtin_fmaf(u[e], sg, sg);
  SCHED();
  hook(IC<1>{});
  SCHED();
#pragma unroll
  for (int e = 0; e < 16; ++e) u[e] = __builtin_amdgcn_rcpf(u[e]);
  SCHED();
#pragma unroll
  for (int e = 0; e < 16; ++e) t[e] = t[e] * u[e];
}
DEVINL v4u tr_frag(LAS unsigned char* L, unsigned a0, unsigned a1) {
  const s16x4 lo = __builtin_amdgcn_ds_read_tr16_b64_v4i16((LAS s16x4*)(L + a0));
  const s16x4 hi = __builtin_amdgcn_ds_read_tr16_b64_v4i16((LAS s16x4*)(L + a1));
  const v2u l2 = __builtin_bit_cast(v2u, lo), h2 = __builtin_bit_cast(v2u, hi);
  return v4u{l2[0], l2[1], h2[0], h2[1]};
}
DEVINL bool wave_all(bool c) { return __builtin_amdgcn_ballot_w64(!c) == 0ull; }
DEVINL float half_sum(float v) {
  const unsigned u = asu(v);
  const auto r = __builtin_amdgcn_permlane32_swap(u, u, false, false);
  const unsigned lo = r[0], hi = r[1];
  return asf(lo) + asf(hi);
}
DEVINL float row_pair_sum(float v) {
  const unsigned u = asu(v);
  const auto r = __builtin_amdgcn_permlane16_swap(u, u, false, false);
  const unsigned lo = r[0], hi = r[1];
  return asf(lo) + asf(hi);
}
DEVINL unsigned fresh_lane() {
  unsigned l;
  asm volatile("v_mbcnt_lo_u32_b32 %0, -1, 0\n\tv_mbcnt_hi_u32_b32 %0, -1, %0" : "=v"(l));
  return l;
}
DEVINL float rev448(float a, float b) {
  const float pr = a * b;
  const float q = __builtin_floorf(pr * (1.0f / 448.0f));
  return __builtin_fmaf(q, -448.0f, pr) * (1.0f / 448.0f);
}

template <int SLEEP> DEVINL void poll_flags(v4u wsr, unsigned voff, bool active, unsigned tag, unsigned code) {
#pragma unroll 1
  for (unsigned spins = 0;; ++spins) {
    unsigned f;
    asm volatile("buffer_load_dword %0, %1, %2, 0 offen sc1\n\ts_waitcnt vmcnt(0)" : "=v"(f) : "v"(voff), "s"(wsr) : "memory");
    if (wave_all(!active || f == tag)) break;
    if (spins > SPIN_CAP) {
      st4_sc1<0>(code, wsr, OFF_DBG, 0);
      break;
    }
    if (SLEEP) __builtin_amdgcn_s_sleep(SLEEP);
  }
}
DEVINL void grp_arrive(LAS unsigned char* L, unsigned ctr) {
  const unsigned a = (unsigned)(unsigned long long)L + ctr;
  asm volatile("s_mov_b64 exec, 1\n\tds_add_u32 %0, %1\n\ts_mov_b64 exec, -1" ::"v"(a), "v"(1u) : "memory");
}
DEVINL void grp_wait(LAS unsigned char* L, unsigned ctr, unsigned count) {
  const unsigned a = (unsigned)(unsigned long long)L + ctr;
#pragma unroll 1
  for (unsigned spins = 0; spins < (1u << 22); ++spins) {
    unsigned v;
    asm volatile("ds_read_b32 %0, %1\n\ts_waitcnt lgkmcnt(0)" : "=v"(v) : "v"(a) : "memory");
    if ((unsigned)__builtin_amdgcn_readfirstlane(v) >= count) break;
  }
}
DEVINL void grp_wait_sleep(LAS unsigned char* L, unsigned ctr, unsigned count) {
  const unsigned a = (unsigned)(unsigned long long)L + ctr;
#pragma unroll 1
  for (unsigned spins = 0; spins < (1u << 20); ++spins) {
    unsigned v;
    asm volatile("ds_read_b32 %0, %1\n\ts_waitcnt lgkmcnt(0)" : "=v"(v) : "v"(a) : "memory");
    if ((unsigned)__builtin_amdgcn_readfirstlane(v) >= count) break;
    __builtin_amdgcn_s_sleep(2);
  }
}
DEVINL void grp_sync(LAS unsigned char* L, unsigned ctr, unsigned count) {
  grp_arrive(L, ctr);
  grp_wait(L, ctr, count);
}
DEVINL bool grp_arrive_last(LAS unsigned char* L, unsigned ctr, unsigned total) {
  const unsigned a = (unsigned)(unsigned long long)L + ctr;
  unsigned v = 1u;
  asm volatile("s_mov_b64 exec, 1\n\tds_add_rtn_u32 %0, %1, %0\n\ts_waitcnt lgkmcnt(0)\n\ts_mov_b64 exec, -1" : "+v"(v) : "v"(a) : "memory");
  return (unsigned)__builtin_amdgcn_readfirstlane(v) == total - 1;
}

#define STAMP(i) do {} while (0)
#define STAMPX(set, i) do {} while (0)
#define STAMP_OUT() do {} while (0)

struct cf { float x, y; };
DEVINL cf cadd(cf a, cf b) { return cf{a.x + b.x, a.y + b.y}; }
DEVINL cf csub(cf a, cf b) { return cf{a.x - b.x, a.y - b.y}; }
template <int SG> DEVINL cf rot90(cf a) { return SG < 0 ? cf{a.y, -a.x} : cf{-a.y, a.x}; }
template <int SG> DEVINL cf ctw(cf a, cf w) {
  return SG < 0 ? cf{a.x * w.x + a.y * w.y, a.y * w.x - a.x * w.y} : cf{a.x * w.x - a.y * w.y, a.y * w.x + a.x * w.y};
}
template <int SG> DEVINL void dft4(cf& a0, cf& a1, cf& a2, cf& a3) {
  const cf s0 = cadd(a0, a2), d0 = csub(a0, a2), s1 = cadd(a1, a3), d1 = rot90<SG>(csub(a1, a3));
  a0 = cadd(s0, s1); a2 = csub(s0, s1); a1 = cadd(d0, d1); a3 = csub(d0, d1);
}
template <int SG> DEVINL void dft8(cf (&a)[8]) {
  cf e0 = a[0], e1 = a[2], e2 = a[4], e3 = a[6], o0 = a[1], o1 = a[3], o2 = a[5], o3 = a[7];
  dft4<SG>(e0, e1, e2, e3);
  dft4<SG>(o0, o1, o2, o3);
  const float h = 0.70710678118654752f;
  const cf r1 = rot90<SG>(o1), r3 = rot90<SG>(o3);
  o1 = cf{o1.x + r1.x, o1.y + r1.y};
  o2 = rot90<SG>(o2);
  o3 = cf{r3.x - o3.x, r3.y - o3.y};
  a[0] = cadd(e0, o0); a[4] = csub(e0, o0);
  a[1] = cf{__builtin_fmaf(o1.x, h, e1.x), __builtin_fmaf(o1.y, h, e1.y)}; a[5] = cf{__builtin_fmaf(o1.x, -h, e1.x), __builtin_fmaf(o1.y, -h, e1.y)};
  a[2] = cadd(e2, o2); a[6] = csub(e2, o2);
  a[3] = cf{__builtin_fmaf(o3.x, h, e3.x), __builtin_fmaf(o3.y, h, e3.y)}; a[7] = cf{__builtin_fmaf(o3.x, -h, e3.x), __builtin_fmaf(o3.y, -h, e3.y)};
}
DEVINL unsigned fsw(unsigned i) { return i ^ (((i >> 4) & 7u) | (((i >> 6) & 1u) << 3)); }
DEVINL void lds_wc(LAS unsigned char* L, unsigned a, cf v) { *(LAS f32x2*)(L + a) = f32x2{v.x, v.y}; }
DEVINL cf lds_rc(LAS unsigned char* L, unsigned a) { const f32x2 v = *(LAS f32x2*)(L + a); return cf{v[0], v[1]}; }
struct FftAddr { unsigned rd[2], wa[8], wb[8]; };
template <int SG> DEVINL void fft512(cf (&v)[8], const cf (&tb)[7], const cf (&tc)[7], LAS unsigned char* L, const FftAddr& A) {
  dft8<SG>(v);
#pragma unroll
  for (int r = 0; r < 8; ++r) lds_wc(L, A.wa[r], v[r]);
  FENCE();
#pragma unroll
  for (int r = 0; r < 8; ++r) v[r] = lds_rc(L, A.rd[r & 1] + 512 * r);
  FENCE();
#pragma unroll
  for (int r = 1; r < 8; ++r) v[r] = ctw<SG>(v[r], tb[r - 1]);
  dft8<SG>(v);
#pragma unroll
  for (int r = 0; r < 8; ++r) lds_wc(L, A.wb[r], v[r]);
  FENCE();
#pragma unroll
  for (int r = 0; r < 8; ++r) v[r] = lds_rc(L, A.rd[r & 1] + 512 * r);
  FENCE();
#pragma unroll
  for (int r = 1; r < 8; ++r) v[r] = ctw<SG>(v[r], tc[r - 1]);
  dft8<SG>(v);
}

constexpr int p2_dma_in(int s) { return (s >= 1 && s + 6 < 32) ? 4 : 0; }
#define GPRE 1
#define NPRE 7
#define BS 4
constexpr int p2_b_in(int s) { return (s >= 5 && ((s - 1) & 3) < 8 / BS && ((s - 1) >> 2) + 1 < 8) ? BS : 0; }
constexpr int p2_pos(int step) {
  int pos = 28 + 16;
  for (int s = 0; s < step; ++s) pos += p2_dma_in(s) + p2_b_in(s);
  return pos;
}
constexpr int p2_dma_done(int u) { return u < NPRE ? 4 * (u + 1) : u < 7 ? 4 * (u + 1) + 16 : p2_pos(u - 5) - p2_b_in(u - 6) / 4; }
constexpr int p2_b_done(int c) { return c < 2 ? 4 * NPRE + 8 * (c + 1) : p2_pos(4 * (c - 1) + 1 + 8 / BS); }

__global__ __launch_bounds__(512, 2) NO_PK void fused_kernel(Params P) {
  __shared__ __attribute__((aligned(16))) unsigned char smem[LDS_BYTES];
  LAS unsigned char* L = (LAS unsigned char*)smem;
  const unsigned lbase = (unsigned)(unsigned long long)L;
  int tid = __builtin_amdgcn_workitem_id_x();
  unsigned lane = tid & 63;
  const int w = __builtin_amdgcn_readfirstlane(tid >> 6);
  STAMP(0);
  unsigned pfd;
  {
    unsigned long long pc;
    unsigned csz;
    asm volatile("s_getpc_b64 %0\ncode_begin_lbl:\n\ts_mov_b32 %1, (code_end_lbl - code_begin_lbl)" : "=s"(pc), "=s"(csz));
    const unsigned o = (unsigned)tid * 128u;
#define PFALL 0
    const unsigned oc = (o < csz && (PFALL || __builtin_amdgcn_workgroup_id_x() >= 224)) ? o : 0u;
    asm volatile("global_load_dword %0, %1, %2" : "=v"(pfd) : "v"(oc), "s"(pc) : "memory");
  }
  const int bid = __builtin_amdgcn_workgroup_id_x();
  const int xcd = bid & 7, rr = bid >> 3;
  const bool is_main = rr < 28;
  const int p = 4 * (xcd >> 1) + (rr & 3);
  const int j = 7 * (xcd & 1) + (rr >> 2);
  const int sidx = xcd * 4 + (rr - 28);
  const v4u wsr = make_rsrc(P.ws, WS_BYTES);
  unsigned tag;
  unsigned h2 = lane >> 5, r32 = lane & 31;
  unsigned trq = (lane & 15) >> 2, trp = lane & 3, trb = (lane >> 4) & 1;
#define RELANE() do { lane = fresh_lane(); tid = w * 64 + lane; h2 = lane >> 5; r32 = lane & 31; trq = (lane & 15) >> 2; trp = lane & 3; trb = (lane >> 4) & 1; } while (0)
#define GET_TAG(ev) do { unsigned e_; asm volatile("v_readlane_b32 %0, %1, 63" : "=s"(e_) : "v"(ev), "v"(pfd) : "memory"); tag = e_ * 1664525u + 1013904223u; } while (0)

  f32x16 a1 = splat16(0.f);
  float gk1 = 0.f, gk2 = 0.f, gsg = 0.f;
  auto finish_z1 = [&](auto&& hook) __attribute__((always_inline)) {
    gelu16(a1, gk1, gk2, gsg, hook);
    v4u o[2];
#pragma unroll
    for (int e = 0; e < 8; ++e) o[e >> 2][e & 3] = pk_bf(a1[2 * e], a1[2 * e + 1]);
    *(LAS v4u*)(L + L_Z1 + w * 2048 + lane * 16) = o[0];
    *(LAS v4u*)(L + L_Z1 + w * 2048 + 1024 + lane * 16) = o[1];
  };
  unsigned yin[8];
  const int slot = is_main ? 16 * j + p : 0;
  const int kw = is_main ? slot : 224;
  const unsigned fft_flags = OFF_FY + (unsigned)(is_main ? j : 0) * 512u + (lane & 15) * 32u;
  auto load_fft_in = [&]() __attribute__((always_inline)) {
    const unsigned so = OFF_YT + (unsigned)slot * 8192u, vo = (tid - 256) * 4u;
    ld4_sc1<0>(yin[0], wsr, vo, so); ld4_sc1<1024>(yin[1], wsr, vo, so); ld4_sc1<2048>(yin[2], wsr, vo, so); ld4_sc1<3072>(yin[3], wsr, vo, so);
    ld4_sc1<0>(yin[4], wsr, vo, so + 4096); ld4_sc1<1024>(yin[5], wsr, vo, so + 4096); ld4_sc1<2048>(yin[6], wsr, vo, so + 4096); ld4_sc1<3072>(yin[7], wsr, vo, so + 4096);
  };
#define FFT_IN_LANDED() do { wait_vm<0>(); asm volatile("" : "+v"(yin[0]), "+v"(yin[1]), "+v"(yin[2]), "+v"(yin[3]), "+v"(yin[4]), "+v"(yin[5]), "+v"(yin[6]), "+v"(yin[7])::"memory"); } while (0)
  const int th = j / 7, dt = j % 7;
  const v4u xr = make_rsrc(P.x, 2048u * 896u);

  if (__builtin_expect(is_main, 1)) {
    const int wm = w & 3, wn = w >> 2;
    const v4u wir = make_rsrc(P.win, 448u * 3584u);
    const int c0 = (j + 2 * p) % 7;
    const unsigned ctab = (unsigned)(0x5432106543210ull >> (4 * c0));
    const unsigned xv0 = (unsigned)(128 * p + 16 * w + (lane >> 3)) * 896u + (((lane & 7) ^ (lane >> 4)) << 4);
    const unsigned xv1 = (xv0 ^ 64u) + 8u * 896u - 1024u;
    const unsigned wv = (unsigned)(8 * w + (lane >> 3)) * 3584u + (unsigned)j * 128u + (((lane & 7) ^ (((lane >> 4) & 1) << 2)) << 4);
    auto issue_x = [&](int i) __attribute__((always_inline)) { dma16x2(xr, xv0, xv1, ((ctab >> (4 * i)) & 7u) * 128u, lbase + (i & 3) * 16384 + w * 2048); };
    auto issue_w2 = [&](int i) __attribute__((always_inline)) { dma16(wir, wv, ((ctab >> (4 * i)) & 7u) * (64u * 3584u) + 1792u, lbase + L_W2 + (i & 3) * 8192 + w * 1024); };
    auto issue_w1 = [&](int i) __attribute__((always_inline)) { dma16(wir, wv, ((ctab >> (4 * i)) & 7u) * (64u * 3584u), lbase + w1_base(i) + w * 1024); };
    STAMPX(3, 1);
    issue_x(0); issue_w2(0);
    STAMPX(3, 2);
#define C1AT -1
#define C2AT 3
    if (C1AT < 0) { issue_x(1); issue_w2(1); }
    STAMPX(3, 3);
    if (tid < 16) *(LAS unsigned*)(L + L_CTR + 4 * tid) = 0u;

    {
      unsigned k = 16 * j + 8 * wn + ((lane & 15) >> 1);
      float ph = (lane & 1) ? 0.25f : 0.0f;
      if (k == 0 && (lane & 1)) { k = 224; ph = 0.0f; }
      const float kf = (float)k, fs = kf * (1.0f / 448.0f), dl = (float)(8 * (lane >> 4) + 32 * (wm & 1));
      const unsigned fo = L_F + (wn * 28 + 2 * wm + (lane >> 5)) * 1024 + (((lane >> 4) & 1) * 32 + (lane & 15)) * 16;
#pragma unroll
      for (int a = 0; a < 4; ++a) {
        if (a < 3 || wm < 2) {
          const unsigned cc = (ctab >> (4 * (2 * a + (wm >> 1)))) & 7u;
          const float fa = rev448(kf, dl + (float)(64 * cc)) + ph;
          float cv[8];
#pragma unroll
          for (int e = 0; e < 8; ++e) cv[e] = __builtin_amdgcn_cosf(__builtin_fmaf((float)e, fs, fa));
          SCHED();
          v4u fh, fl;
#pragma unroll
          for (int e = 0; e < 4; ++e) {
            const unsigned h0 = asu(cv[2 * e]) & 0xffff0000u, h1 = asu(cv[2 * e + 1]) & 0xffff0000u;
            fh[e] = __builtin_amdgcn_perm(h1, h0, 0x07060302u);
            fl[e] = pk_bf(cv[2 * e] - asf(h0), cv[2 * e + 1] - asf(h1));
          }
          *(LAS v4u*)(L + fo + a * 8192) = fh;
          *(LAS v4u*)(L + fo + a * 8192 + 256) = fl;
          SCHED();
        }
        if (a == C1AT) { issue_x(1); issue_w2(1); }
        if (a == C2AT) { issue_x(2); issue_w2(2); }
        STAMPX(3, 4 + a);
      }
    }
#define W1LATE 0
#define SMALLAT 1
    unsigned epoch_v;
    auto small_loads = [&]() __attribute__((always_inline)) {
    dma4(make_rsrc(P.bsp, 4096u), lane * 4u, (unsigned)p * 256u, lbase + L_BSP);
    {
      const unsigned wb_ = w == 0 ? (STWARM ? OFF_ST + (unsigned)(p * 14 + j) * 2048u : 0u) : w == 1 ? OFF_FA + (unsigned)j * 512u + (lane & 4) * (OFF_FY - OFF_FA) / 4 : w == 2 ? OFF_FH + (unsigned)p * 512u : w == 3 ? OFF_FW : OFF_FZ + (unsigned)(w - 4) * 1024u;
      const unsigned wm_ = w == 0 ? (STWARM ? 15u : 0u) : w < 3 ? 3u : 7u;
      const unsigned eo = lane == 63 ? 0u : wb_ + (lane & wm_) * 128u;
      asm volatile("buffer_load_dword %0, %1, %2, 0 offen sc1" : "=v"(epoch_v) : "v"(eo), "s"(wsr) : "memory");
    }
    };
    if (!SMALLAT) small_loads();
#define NSMALL 2
    STAMP(1);

    unsigned aoff[4], boff[4];
#pragma unroll
    for (int r = 0; r < 4; ++r) {
      aoff[r] = (32 * wm + r32) * 128 + (((2 * (r ^ wm) + h2) ^ ((r32 >> 1) & 7)) << 4);
      boff[r] = L_W2 + (16 * (r ^ wm) + 8 * h2 + trq) * 128 + ((wn ^ (trq >> 1)) << 6) + 32 * trb + 8 * trp;
      PIN(aoff[r]);
      PIN(boff[r]);
    }
    unsigned soff[2];
    soff[0] = (16 * w + (lane & 15)) * 128 + (((lane >> 4) ^ ((lane & 15) >> 1)) << 4);
    soff[1] = soff[0] ^ 64u;
    PIN(soff[0]); PIN(soff[1]);
    v4u xa[7][4];
    v4u wb[2][4], xs[2][2];
    f32x16 a2 = splat16(0.f);
    f32x4 sx1 = f32x4{0.f, 0.f, 0.f, 0.f}, sx2 = sx1, scs = sx1;
    v4u ones = v4u{0x3f803f80u, 0x3f803f80u, 0x3f803f80u, 0x3f803f80u};
    const unsigned selv = ((lane & 15) == ((lane >> 4) & 1)) ? 0x3f803f80u : 0u;
    v4u sel = v4u{selv, selv, selv, selv};
    PIN(ones); PIN(sel);
    const unsigned rowx = L_XS + (32 * wm + r32) * 8;
    const unsigned colw = (32 * wn + (lane & 15)) * 16 + wm * 4, colr = (32 * wn + r32) * 16 + h2 * 8;
#pragma unroll
    for (int i = 0; i < 7; ++i) {
      const int c = i - 1;
      if (!SMALLAT) { if (i < 3) wait_vm<6 + NSMALL>(); else if (i < 5) wait_vm<6>(); else if (i == 5) wait_vm<5>(); else wait_vm<4>(); }
      else if (!W1LATE) { if (i < 4) wait_vm<6>(); else if (i == 4) wait_vm<8>(); else if (i == 5) wait_vm<7>(); else wait_vm<6>(); }
      else { if (i < 4) wait_vm<6>(); else if (i == 4) wait_vm<8>(); else if (i == 5) wait_vm<5>(); else wait_vm<2>(); }
      STAMPX(2, 1 + 2 * i);
      bar();
      STAMPX(2, 2 + 2 * i);
      if (i == 0) { STAMP(2); STAMPX(3, 9); }
      if (i == 3 && !SMALLAT) GET_TAG(epoch_v);
#pragma unroll
      for (int r = 0; r < 4; ++r) {
        SCHED();
        if (i >= 1) a2 = mfma32(wb[c & 1][r], xa[c][r], a2);
        SCHED();
        if (r < 2) xs[i & 1][r] = *(LAS v4u*)(L + soff[r] + (i & 3) * 16384);
        wb[i & 1][r] = tr_frag(L, boff[r] + (i & 3) * 8192, boff[r] + (i & 3) * 8192 + 512);
        SCHED();
        if (i >= 1) {
          if (r & 1) sx2 = mfma16(xs[c & 1][r >> 1], xs[c & 1][r >> 1], sx2); else sx1 = mfma16(ones, xs[c & 1][r >> 1], sx1);
          if (r == 3) scs = mfma16(sel, wb[c & 1][0], scs);
        }
        SCHED();
        xa[i][r] = *(LAS v4u*)(L + aoff[r] + (i & 3) * 16384);
        SCHED();
        if (i <= 3) { if (r == 0) issue_x(i + 3); if (r == 2) issue_w2(i + 3); }
        if (!W1LATE && i >= 4 && (r & 1)) issue_w1(2 * (i - 4) + (r >> 1));
      }
      SCHED();
      if (SMALLAT && i == 3) small_loads();
    }
    sx1 = mfma16(ones, xs[0][0], sx1);
    sx2 = mfma16(xs[0][0], xs[0][0], sx2);
    scs = mfma16(sel, wb[0][0], scs);
    sx1 = mfma16(ones, xs[0][1], sx1);
    sx2 = mfma16(xs[0][1], xs[0][1], sx2);
    SCHED();
    a2 = mfma32(wb[0][0], xa[6][0], a2);
    a2 = mfma32(wb[0][1], xa[6][1], a2);
    SCHED();
    {
      const float d01 = (lane & 1) ? sx2[1] : sx2[0], d23 = (lane & 1) ? sx2[3] : sx2[2];
      const float dg = (lane & 2) ? d23 : d01;
      const float nm = sx1[0] * (-1.0f / 448.0f);
      const float ve = __builtin_fmaxf(dg * (1.0f / 448.0f) - nm * nm, 0.f) + 1e-5f;
      if (((lane & 15) >> 2) == (lane >> 4)) *(LAS f32x2*)(L + L_XS + (16 * w + (lane & 15)) * 8) = f32x2{nm, ve};
      if (lane < 16) {
        *(LAS float*)(L + L_CS2 + colw) = scs[0];
        *(LAS float*)(L + L_CS2 + colw + 256) = scs[1];
      }
    }
    SCHED();
    a2 = mfma32(wb[0][2], xa[6][2], a2);
    a2 = mfma32(wb[0][3], xa[6][3], a2);
    SCHED();
    STAMP(3);
    STAMPX(2, 15);
    bar();
    float nmu, rho;
    unsigned fv[4];
#pragma unroll
    for (int r = 0; r < 4; ++r) fv[r] = L_F + wn * 28672 + lane * 16 + (r ^ wm) * 1024;
    {
      const f32x2 t0 = *(LAS f32x2*)(L + rowx);
      const f32x2 c2 = *(LAS f32x2*)(L + L_CS2 + colr);
      if (!W1LATE) issue_w1(6);
#pragma unroll
      for (int r = 0; r < 4; ++r) wb[0][r] = *(LAS v4u*)(L + fv[r]);
      nmu = t0[0];
      a2 = __builtin_amdgcn_mfma_f32_32x32x2f32(c2[0] + c2[1], nmu, a2, 0, 0, 0);
      const float ve = t0[1];
      rho = __builtin_amdgcn_rsqf(ve);
      gsg = ve * rho;
      gk1 = -2.302208198f * rho;
      gk2 = -0.1029432397f * rho * rho * rho;
      gelu16(a2, gk1, gk2, gsg, [&](auto hc) __attribute__((always_inline)) { if (W1LATE == 2) issue_w1(5 + decltype(hc)::value); });
      float s1a = a2[0], s1b = a2[1], s2a = a2[0] * a2[0], s2b = a2[1] * a2[1];
#pragma unroll
      for (int e = 2; e < 16; e += 2) {
        s1a += a2[e]; s1b += a2[e + 1];
        s2a = __builtin_fmaf(a2[e], a2[e], s2a); s2b = __builtin_fmaf(a2[e + 1], a2[e + 1], s2b);
      }
      const auto sw = __builtin_amdgcn_permlane32_swap(asu(s1a + s1b), asu(s2a + s2b), false, false);
      const unsigned sw0 = sw[0], sw1 = sw[1];
      const float part = asf(sw0) + asf(sw1);
      if (wn) *(LAS float*)(L + L_PS + wm * 256 + lane * 4) = part;
      if (SMALLAT) { wait_vm<(W1LATE == 1 ? 0 : W1LATE == 2 ? 2 : 7)>(); GET_TAG(epoch_v); }
      bar();
      if (!wn) {
        const float tot = part + *(LAS float*)(L + L_PS + wm * 256 + lane * 4);
        st8_sc1<0>(v2u{asu(tot), tag}, wsr, r32 * 16u + h2 * 8u, OFF_ST + (unsigned)((p * 14 + j) * 128 + 32 * wm) * 16u);
      }
    }
    STAMP(4);
    const unsigned gso = OFF_ST + (unsigned)(p * 14) * 2048u;
    const unsigned gvo = (lane >> 4) * 2048u + (16 * w + (lane & 15)) * 16u, gv3 = gvo & ~4096u;
    v4u gq[4];
    auto issue_gather = [&]() __attribute__((always_inline)) {
#pragma unroll
      for (int a = 0; a < 4; ++a) ld16_sc1<0>(gq[a], wsr, a == 3 ? gv3 : gvo, gso + a * 8192);
    };
    unsigned yq[4];
    {
      f32x16 ay = splat16(0.f);
#pragma unroll
      for (int i = 1; i <= 7; ++i) {
#pragma unroll
        for (int r = 0; r < 4; ++r) {
          SCHED();
          ay = mfma32(wb[(i - 1) & 1][r], xa[i - 1][r], ay);
          SCHED();
          if (i < 7) wb[i & 1][r] = *(LAS v4u*)(L + fv[r] + i * 4096);
          if (W1LATE && i <= 4 && (r & 1) && 2 * (i - 1) + (r >> 1) < (W1LATE == 2 ? 5 : 7)) issue_w1(2 * (i - 1) + (r >> 1));
          if (i == GATH) ld16_sc1<0>(gq[r], wsr, r == 3 ? gv3 : gvo, gso + r * 8192);
        }
        SCHED();
      }
      const float dc = (j == 0 && wn == 0 && h2 == 0) ? 448.0f * nmu : 0.0f;
      yq[0] = pk_h((ay[0] + ay[8] + dc) * rho, (ay[1] + ay[9]) * rho);
#pragma unroll
      for (int q = 1; q < 4; ++q) yq[q] = pk_h((ay[2 * q] + ay[2 * q + 8]) * rho, (ay[2 * q + 1] + ay[2 * q + 9]) * rho);
      const unsigned yso = OFF_YT + (unsigned)(16 * j + 8 * wn) * 8192u + (unsigned)(128 * p + 32 * wm) * 4u;
      const unsigned yvo = h2 * 16384u + r32 * 4u;
      st4_sc1<0>(yq[0], wsr, yvo, yso);
      st4_sc1<0>(yq[1], wsr, yvo, yso + 8192);
      st4_sc1<0>(yq[2], wsr, yvo, yso + 32768);
      st4_sc1<0>(yq[3], wsr, yvo, yso + 40960);
    }
    STAMP(5);
    {
      wait_vm<4>();
#pragma unroll 1
      for (unsigned spins = 0;; ++spins) {
        asm volatile("" : "+v"(gq[0]), "+v"(gq[1]), "+v"(gq[2]), "+v"(gq[3])::"memory");
        unsigned bad = 0;
#pragma unroll
        for (int a = 0; a < 4; ++a) bad |= (gq[a][1] ^ tag) | (gq[a][3] ^ tag);
        if (wave_all(bad == 0)) break;
        if (spins > SPIN_CAP) { st4_sc1<0>(1u, wsr, OFF_DBG, 0); break; }
        issue_gather();
        wait_vm<0>();
      }
      STAMPX(1, 14);
      const float s1 = (asf(gq[0][0]) + asf(gq[1][0])) + (asf(gq[2][0]) + (lane < 32 ? asf(gq[3][0]) : 0.f));
      const float s2 = (asf(gq[0][2]) + asf(gq[1][2])) + (asf(gq[2][2]) + (lane < 32 ? asf(gq[3][2]) : 0.f));
      const auto sw = __builtin_amdgcn_permlane32_swap(asu(s1), asu(s2), false, false);
      const unsigned sw0 = sw[0], sw1 = sw[1];
      const float tot = row_pair_sum(asf(sw0) + asf(sw1));
      *(LAS float*)(L + L_ZS + (16 * w + (lane & 15)) * 8 + h2 * 4) = tot;
    }
    bar();
    {
      const f32x2 t0 = *(LAS f32x2*)(L + rowx + (L_ZS - L_XS));
      const float mean = t0[0] * (1.0f / 896.0f);
      const float rs = __builtin_amdgcn_rsqf(__builtin_fmaxf(t0[1] * (1.0f / 896.0f) - mean * mean, 0.f) + 1e-5f);
      const float mo = -mean * rs;
      const unsigned tw = L_Z1 + w * 2048 + r32 * 64;
#pragma unroll
      for (int g = 0; g < 4; ++g) {
        v2u o;
        o[0] = pk_bf(__builtin_fmaf(a2[4 * g], rs, mo), __builtin_fmaf(a2[4 * g + 1], rs, mo));
        o[1] = pk_bf(__builtin_fmaf(a2[4 * g + 2], rs, mo), __builtin_fmaf(a2[4 * g + 3], rs, mo));
        *(LAS v2u*)(L + tw + (((2 * g + h2) ^ ((r32 >> 1) & 7)) << 3)) = o;
      }
      FENCE();
      const unsigned tr0 = L_Z1 + w * 2048 + (8 * h2 + trq) * 64 + (((4 * trb + trp) ^ (4 * h2 + (trq >> 1))) << 3);
      const unsigned tr1 = (tr0 ^ 16u) + 256;
      const v4u z0 = tr_frag(L, tr0, tr1), z1f = tr_frag(L, tr0 + 1024, tr1 + 1024);
      const unsigned zso = OFF_Z2N + (unsigned)((j * 128 + 8 * p + 2 * wm) * 2 + wn) * 1024u;
      st16_sc1<0>(z0, wsr, lane * 16u, zso);
      st16_sc1<2048>(z1f, wsr, lane * 16u, zso);
    }
    STAMP(6);
    wait_vm<2>();
    if (grp_arrive_last(L, L_CTR + 44, 8u)) {
      if (lane == 0) st4_sc1<0>(tag, wsr, OFF_FY + (unsigned)(j * 16 + p) * 32u, 0);
    }
    auto z1_loop = [&](auto&& hook) __attribute__((always_inline)) {
      scs = f32x4{0.f, 0.f, 0.f, 0.f};
#pragma unroll
      for (int r = 0; r < 4; ++r) { boff[r] -= L_W2; PIN(boff[r]); }
      static_for<0, 32>([&](auto tc) __attribute__((always_inline)) {
        constexpr int t = decltype(tc)::value, i = t >> 2, r = t & 3, c = i - 1;
        SCHED();
        if constexpr (i >= 1) {
          a1 = mfma32(wb[c & 1][r], xa[c][r], a1);
          if constexpr (r == 0) scs = mfma16(sel, wb[c & 1][0], scs);
        }
        SCHED();
        if constexpr (i < 7) wb[i & 1][r] = tr_frag(L, boff[r] + w1_base(i), boff[r] + w1_base(i) + 512);
        SCHED();
        hook(tc);
      });
      if (lane < 16) {
        *(LAS float*)(L + L_CS1 + colw) = scs[0];
        *(LAS float*)(L + L_CS1 + colw + 256) = scs[1];
      }
      bar();
      STAMP(8);
      const f32x2 c2 = *(LAS f32x2*)(L + L_CS1 + colr);
      a1 = __builtin_amdgcn_mfma_f32_32x32x2f32(c2[0] + c2[1], nmu, a1, 0, 0, 0);
    };
    auto publish_z2n = [&]() __attribute__((always_inline)) {
      if (grp_arrive_last(L, L_CTR + 16, 8u)) {
        if (lane == 0) st4_sc1<0>(tag, wsr, OFF_FA + (unsigned)(j * 16 + p) * 32u, 0);
      }
      STAMP(7);
    };
#define YASYNC 1
    if (w >= 4) {
      unsigned yff = 0;
      z1_loop([&](auto tc) __attribute__((always_inline)) {
        constexpr int t = decltype(tc)::value;
        if constexpr (t == FLAGT) { wait_vm<0>(); publish_z2n(); }
        if constexpr (t == YLT - 8) ld4_sc1<0>(yff, wsr, fft_flags, 0);
        if constexpr (t == YLT) { wait_vm<0>(); unsigned& y_ = yff; PIN(y_); if (!wave_all(yff == tag)) poll_flags<1>(wsr, fft_flags, true, tag, 3u); load_fft_in(); }
      });
      FFT_IN_LANDED();
    }
    int w_opaque = w;
    asm volatile("" : "+s"(w_opaque));
    if (w_opaque < 4) {
#define ROTMASK 0
      const unsigned rot = (j + 2 * p) & ROTMASK;
      const v4u spr = make_rsrc(P.wsp, 2048u * 4096u);
      const unsigned spv0 = (unsigned)(128 * p + (lane >> 3)) * 4096u + (((lane & 7) ^ (lane >> 4)) << 4);
      const unsigned spv1 = (spv0 ^ 64u) + 8u * 4096u - 1024u, spv2 = spv0 + 16u * 4096u - 2048u, spv3 = (spv0 ^ 64u) + 24u * 4096u - 3072u;
      auto sp_soff = [&](int u) __attribute__((always_inline)) { return (unsigned)w * 1024u + (((u >> 2) ^ rot) * 128) + (unsigned)((u + 1 + w) & 3) * (32 * 4096); };
      auto issue_sp = [&](int u) __attribute__((always_inline)) { dma16x4(spr, spv0, spv1, spv2, spv3, sp_soff(u), lbase + ring_base(u % 7) + w * 4096); };
      z1_loop([&](auto tc) __attribute__((always_inline)) {
        constexpr int t = decltype(tc)::value, q = t - 4;
        if constexpr (q >= 0 && q < 12) {
          if constexpr ((q & 3) == 0) set_m0(lbase + ring_base(q >> 2) + w * 4096);
          dma_piece<(q & 3) * 1024>(spr, (q & 3) == 0 ? spv0 : (q & 3) == 1 ? spv1 : (q & 3) == 2 ? spv2 : spv3, sp_soff(q >> 2));
        }
        if constexpr (t == FLAGT) { wait_vm<12>(); publish_z2n(); }
      });
      if (NPRE > 3) issue_sp(3);
      if (NPRE > 6) issue_sp(4);
      finish_z1([&](auto hc) __attribute__((always_inline)) { constexpr int u = (NPRE > 6 ? 5 : 4) + decltype(hc)::value; if constexpr (u < NPRE) issue_sp(u); });
      __builtin_amdgcn_s_setprio(GPRIO_);
      unsigned rdl[4], rdh[4];
#pragma unroll
      for (int ks = 0; ks < 4; ++ks) {
        rdl[ks] = w * 4096 + r32 * 128 + (((2 * ks + h2) ^ ((r32 >> 1) & 7)) << 4);
        rdh[ks] = rdl[ks] + RING_HI;
        PIN(rdl[ks]);
        PIN(rdh[ks]);
      }
      poll_flags<ASL>(wsr, OFF_FA + (unsigned)(j * 4 + w) * 128u + (lane & 3) * 32u, true, tag, 2u);
      STAMP(10);
      const unsigned zso = OFF_Z2N + (unsigned)(j * 128 + 32 * w) * 2048u;
      const unsigned zv = lane * 16u;
      v4u bz[2][8];
      auto issue_b = [&](int c) __attribute__((always_inline)) {
        v4u(&d)[8] = bz[c & 1];
        const unsigned so = zso + (c ^ rot) * 8192;
        ld16_sc1<0>(d[0], wsr, zv, so); ld16_sc1<1024>(d[1], wsr, zv, so); ld16_sc1<2048>(d[2], wsr, zv, so); ld16_sc1<3072>(d[3], wsr, zv, so);
        ld16_sc1<0>(d[4], wsr, zv, so + 4096); ld16_sc1<1024>(d[5], wsr, zv, so + 4096); ld16_sc1<2048>(d[6], wsr, zv, so + 4096); ld16_sc1<3072>(d[7], wsr, zv, so + 4096);
      };
      issue_b(0);
      issue_b(1);
      static_for<NPRE, 7>([&](auto uc) __attribute__((always_inline)) { issue_sp(decltype(uc)::value); });
      STAMP(9);
      f32x16 acc[2][4];
      {
        const float bias = bf_lo(*(LAS unsigned short*)(L + L_BSP + (32 * w + r32) * 2));
        acc[0][3] = splat16(bias);
        acc[1][3] = splat16(bias);
#pragma unroll
        for (int b = 0; b < 3; ++b) { acc[0][b] = splat16(0.f); acc[1][b] = splat16(0.f); }
      }
      v4u af[2][4];
      unsigned dmp;
      static_for<0, 33>([&](auto sc) __attribute__((always_inline)) {
        constexpr int s = decltype(sc)::value;
        constexpr int u = s - 1, c = u >> 2, rg = u & 3;
        v4u(&d)[8] = bz[c & 1];
        v4u(&dn)[8] = bz[(c + 1) & 1];
        if constexpr (s >= 1) asm volatile("" : "+v"(af[u & 1][0]), "+v"(af[u & 1][1]), "+v"(af[u & 1][2]), "+v"(af[u & 1][3])::"memory");
        if constexpr (s < 32) {
          constexpr int need = p2_pos(s) - p2_dma_done(s);
          asm volatile("s_waitcnt vmcnt(%0)" ::"i"(need > 63 ? 63 : need) : "memory");
          constexpr unsigned rb = ring_base(s % 7);
#pragma unroll
          for (int ks = 0; ks < 4; ++ks) af[s & 1][ks] = *(LAS v4u*)(L + (rb >= RING_HI ? rdh[ks] + (rb - RING_HI) : rdl[ks] + rb));
        }
        if constexpr (s == 28) dmp = w * 4096 + fresh_lane() * 16;
        if constexpr (s == A3S || s == 29) grp_arrive(L, L_CTR + 24 + 4 * w);
        SCHED();
        if constexpr (s >= 1) {
          if constexpr (rg == 0) {
            constexpr int need = p2_pos(s) - p2_b_done(c);
            asm volatile("s_waitcnt vmcnt(%8)"
                         : "+v"(d[0]), "+v"(d[1]), "+v"(d[2]), "+v"(d[3]), "+v"(d[4]), "+v"(d[5]), "+v"(d[6]), "+v"(d[7])
                         : "i"(need > 63 ? 63 : need)
                         : "memory");
          }
          const unsigned so = sp_soff(u + 7);
          const unsigned bo = zso + ((c + 1) ^ rot) * 8192 + rg * 4096;
          if constexpr (p2_dma_in(s)) set_m0(lbase + ring_base(u % 7) + w * 4096);
          static_for<0, 4>([&](auto kc) __attribute__((always_inline)) {
            constexpr int ks = decltype(kc)::value;
            acc[0][rg] = mfma32(d[2 * ks], af[u & 1][ks], acc[0][rg]);
            SCHED();
            if constexpr (p2_dma_in(s)) dma_piece<ks * 1024>(spr, ks == 0 ? spv0 : ks == 1 ? spv1 : ks == 2 ? spv2 : spv3, so);
            SCHED();
            acc[1][rg] = mfma32(d[2 * ks + 1], af[u & 1][ks], acc[1][rg]);
            SCHED();
            if constexpr (p2_b_in(s) == 4) ld16_sc1<ks * 1024>(dn[4 * rg + ks], wsr, zv, bo);
            if constexpr (p2_b_in(s) == 8) { ld16_sc1<(ks & 1) * 2048>(dn[2 * ks], wsr, zv, bo + (ks >> 1) * 4096); ld16_sc1<(ks & 1) * 2048 + 1024>(dn[2 * ks + 1], wsr, zv, bo + (ks >> 1) * 4096); }
            if constexpr (s >= 30) {
              const f32x16& t = acc[ks >> 1][rg - 1];
              constexpr int o = 8 * (ks & 1);
              *(LAS v4u*)(L + dmp + ring_base(rg) + ks * 1024) =
                  v4u{pk_bf(t[o], t[o + 1]), pk_bf(t[o + 2], t[o + 3]), pk_bf(t[o + 4], t[o + 5]), pk_bf(t[o + 6], t[o + 7])};
            }
            SCHED();
          });
        }
      });
      grp_arrive(L, L_CTR + 0);
      STAMP(11);
      STAMPX(4, 1);
      RELANE();
      v4u es[2];
      {
        const unsigned one = (r32 & 1) ? 0x3f800000u : 0x00003f80u;
        const unsigned slot = ((r32 >> 2) & 1) == h2 ? ((r32 >> 2) & 6) + ((r32 >> 1) & 1) : 8u;
#pragma unroll
        for (int e = 0; e < 8; ++e) es[e >> 2][e & 3] = slot == (unsigned)e ? one : 0u;
      }
      unsigned pa[3], za = L_Z1 + w * 2048 + lane * 16, fo_h;
#pragma unroll
      for (int rel = 1; rel < 4; ++rel) { pa[rel - 1] = ring_base(rel) + ((w - rel) & 3) * 4096 + lane * 16; PIN(pa[rel - 1]); }
      {
        const int k0 = (4 * j) / 14, k1 = (4 * j + 3) / 14;
        const unsigned f0 = OFF_FH + (unsigned)(p * 4 + k0) * 128u + (unsigned)(j - (7 * k0) / 2) * 32u, f1 = OFF_FH + (unsigned)(p * 4 + k1) * 128u + (unsigned)(j - (7 * k1) / 2) * 32u;
        fo_h = lane ? f1 : f0;
      }
      PIN(es[0]); PIN(es[1]); PIN(za); PIN(fo_h);
      const unsigned hso = OFF_H + (unsigned)((p * 4 + w) * 56 + 4 * j) * 1024u;
      grp_wait(L, L_CTR + 0, 8u);
      STAMP(19);
      {
        v4u pt[3][4], z1q[4];
#pragma unroll
        for (int q = 0; q < 4; ++q) {
#pragma unroll
          for (int rel = 1; rel < 4; ++rel) pt[rel - 1][q] = *(LAS v4u*)(L + pa[rel - 1] + q * 1024);
          if (q == 1) {
#pragma unroll
            for (int a = 0; a < 4; ++a) z1q[a] = *(LAS v4u*)(L + za + (a >> 1) * 8192 + (a & 1) * 1024);
          }
        }
        grp_arrive(L, L_CTR + 20);
        SCHED();
        float zf[32];
#pragma unroll
        for (int q = 0; q < 4; ++q)
#pragma unroll
          for (int rel = 0; rel < 3; ++rel) {
            acc[q >> 1][3] = mfma32(es[q & 1], pt[rel][q], acc[q >> 1][3]);
            SCHED();
            if (3 * q + rel >= 4) {
              const int a = 3 * q + rel - 4;
#pragma unroll
              for (int e = 0; e < 2; ++e) {
                zf[4 * a + 2 * e] = bf_lo(z1q[a >> 1][2 * (a & 1) + e]); zf[4 * a + 2 * e + 1] = bf_hi(z1q[a >> 1][2 * (a & 1) + e]);
                PIN(zf[4 * a + 2 * e]); PIN(zf[4 * a + 2 * e + 1]);
              }
            }
            SCHED();
          }
        static_for<0, 4>([&](auto qc) __attribute__((always_inline)) {
          constexpr int q = decltype(qc)::value;
          const f32x16& t = acc[q >> 1][3];
          v4u hq;
#pragma unroll
          for (int e = 0; e < 4; ++e) hq[e] = pk_bf(zf[8 * q + 2 * e] * t[8 * (q & 1) + 2 * e], zf[8 * q + 2 * e + 1] * t[8 * (q & 1) + 2 * e + 1]);
          st16_sc1<q * 1024>(hq, wsr, lane * 16u, hso);
          SCHED();
        });
      }
      STAMP(12);
      STAMPX(4, 2);
      wait_vm<0>();
      if (grp_arrive_last(L, L_CTR + 4, 4u)) {
        if (lane < 2) st4_sc1<0>(tag, wsr, fo_h, 0);
      }
      __builtin_amdgcn_s_setprio(0);
      STAMP(13);
      asm volatile("" : "=v"(a1), "=v"(gk1), "=v"(gk2), "=v"(gsg));
      asm volatile("" : "=v"(yin[0]), "=v"(yin[1]), "=v"(yin[2]), "=v"(yin[3]), "=v"(yin[4]), "=v"(yin[5]), "=v"(yin[6]), "=v"(yin[7]));
    }
  } else {
    if (tid < 16) *(LAS unsigned*)(L + L_CTR + 4 * tid) = 0u;
    const bool wworker = (rr ^ xcd) & 1;
    {
      unsigned epoch_v;
      asm volatile("buffer_load_dword %0, off, %1, 0 sc1" : "=v"(epoch_v) : "s"(wsr) : "memory");
      if (wworker) {
        const int rh = sidx >> 4, lb = (sidx >> 1) & 7;
        const unsigned v0 = lane * 16u;
        const v4u rs = make_rsrc(w < 4 ? P.wr : P.wi, 2048u * 450u);
        const unsigned so = (unsigned)(32 * lb + 256 * (4 * rh + (w & 3))) * 450u;
#pragma unroll
        for (int a = 0; a < 3; ++a) dma16x4(rs, v0, v0, v0, v0, so + a * 4096, lbase + w * 16384 + a * 4096);
        dma16x2(rs, v0, v0, so + 12288, lbase + w * 16384 + 12288);
        dma16(rs, v0, so + 14336, lbase + w * 16384 + 14336);
        wait_vm<0>();
        bar();
        const unsigned q = (tid >> 3) & 3, li = tid & 7;
        const unsigned a0 = (4 * li + q) * 450, a1 = a0 + 65536;
#pragma unroll 1
        for (int hf = 0; hf < 3; ++hf) {
          unsigned short e[5][8];
          unsigned kk[5];
#pragma unroll
          for (int it = 0; it < 5; ++it) {
            kk[it] = (tid >> 5) + 16 * (5 * hf + it);
            kk[it] = kk[it] < 225 ? kk[it] : 224;
#pragma unroll
            for (int r = 0; r < 8; ++r) e[it][r] = *(LAS unsigned short*)(L + (r < 4 ? a0 : a1) + kk[it] * 2 + (r & 3) * 16384);
          }
          SCHED();
#pragma unroll
          for (int it = 0; it < 5; ++it) {
            v4u o;
#pragma unroll
            for (int r = 0; r < 4; ++r) o[r] = (unsigned)e[it][r] | ((unsigned)e[it][4 + r] << 16);
            st16_sc1<0>(o, wsr, (((kk[it] * 4 + q) * 2 + rh) * 64 + 8 * lb + li) * 16u, OFF_WT);
          }
        }
      }
      wait_vm<0>();
      GET_TAG(epoch_v);
      bar();
      if (tid == 0) st4_sc1<0>(tag, wsr, OFF_FW + (unsigned)sidx * 32u, 0);
      bar();
      STAMP(8);
    }
    const bool nyq = sidx == 1;
    if (!nyq || w < 4) {
      unsigned dmy = 0;
      const v4u spr = make_rsrc(P.wsp, 2048u * 4096u);
      const unsigned vo = lane * 4096u, so = (unsigned)(128 * p + 64 * (w >> 2)) * 4096u + (unsigned)(w & 3) * 1024u;
#pragma unroll
      for (int i = 0; i < 8; ++i) {
        asm volatile("buffer_load_dword %0, %1, %2, %3 offen" : "+v"(dmy) : "v"(vo), "s"(spr), "s"(so + (((wworker ? LATE0 : 0) + i) & 7) * 128u) : "memory");
        if (i == 7 - LATE0 && wworker) {
          const v4u wor = make_rsrc(P.wout, 896u * 896u);
          asm volatile("buffer_load_dword %0, %1, %2, %3 offen" : "+v"(dmy) : "v"(lane * 128u), "s"(wor), "s"((unsigned)((sidx >> 1) * 8 + w) * 8192u) : "memory");
        }
      }
      STAMP(9);
#define WOL2 1
      {
        asm volatile("s_waitcnt vmcnt(0)" : "+v"(dmy)::"memory");
        const v4u wor = make_rsrc(P.wout, 896u * 896u);
#pragma unroll
        for (int i = 0; i < 3; ++i)
          asm volatile("buffer_load_dword %0, %1, %2, %3 offen" : "+v"(dmy) : "v"(lane * 128u), "s"(wor), "s"((unsigned)(((rr & 3) * 8 + w) * 3 + i) * 8192u) : "memory");
      }
      asm volatile("s_waitcnt vmcnt(0)" : "+v"(dmy)::"memory");
      STAMP(10);
      STAMP_OUT();
      return;
    }
    poll_flags<8>(wsr, fft_flags, true, tag, 3u);
    load_fft_in();
    FFT_IN_LANDED();
  }

  if (w >= 4) {
#define FPRIO 2
#define FPRIO_C 0
#define FPRIO_A 0
#define FPRIO_G 0
    __builtin_amdgcn_s_setprio(FPRIO);
    const int fw = w - 4;
    const unsigned tp = tid - 256;
    const int mode = is_main ? (slot == 0 ? 1 : 0) : 2;
    const unsigned myreg = L_FFT + 4096u * fw;
    unsigned fwf;
    ld4_sc1<0>(fwf, wsr, OFF_FW + (lane & 31) * 32u, 0);
    cf t1[8], tb[7], tc[7];
    FftAddr FA;
    {
      const float a1 = (float)(fw * lane) * (1.0f / 2048.0f), d1 = (float)fw * (1.0f / 32.0f);
      const float ab = (float)(lane & 7) * (1.0f / 64.0f), ac = (float)lane * (1.0f / 512.0f);
#pragma unroll
      for (int r = 0; r < 8; ++r) {
        const float a = __builtin_fmaf((float)r, d1, a1);
        t1[r] = cf{__builtin_amdgcn_cosf(a) * 0.022097086912079612f, __builtin_amdgcn_sinf(a) * 0.022097086912079612f};
        PIN(t1[r].x); PIN(t1[r].y);
        FA.wa[r] = myreg + 8 * fsw(8 * lane + r);
        FA.wb[r] = myreg + 8 * fsw((lane >> 3) * 64 + (lane & 7) + 8 * r);
        PIN(FA.wa[r]); PIN(FA.wb[r]);
      }
#pragma unroll
      for (int r = 1; r < 8; ++r) {
        const float a = r * ab, b = r * ac;
        tb[r - 1] = cf{__builtin_amdgcn_cosf(a), __builtin_amdgcn_sinf(a)};
        tc[r - 1] = cf{__builtin_amdgcn_cosf(b), __builtin_amdgcn_sinf(b)};
        PIN(tb[r - 1].x); PIN(tb[r - 1].y); PIN(tc[r - 1].x); PIN(tc[r - 1].y);
      }
      FA.rd[0] = myreg + 8 * fsw(lane);
      FA.rd[1] = myreg + 8 * fsw(lane + 64) - 512;
      PIN(FA.rd[0]); PIN(FA.rd[1]);
    }
    unsigned e1 = L_FFT + 8 * fsw(tp), e2a = L_FFT + 8 * fsw(2 * tp), e2b = e2a ^ 8u;
    PIN(e1); PIN(e2a); PIN(e2b);
    wait_vm<0>();
    PIN(fwf);
    if (!wave_all(fwf == tag)) poll_flags<2>(wsr, OFF_FW + (lane & 31) * 32u, true, tag, 3u);
    v4u wq[2];
    ld16_sc1<0>(wq[0], wsr, lane * 16u, OFF_WT + (unsigned)(kw * 4 + fw) * 2048u);
    ld16_sc1<1024>(wq[1], wsr, lane * 16u, OFF_WT + (unsigned)(kw * 4 + fw) * 2048u);
    STAMP(10);
    __builtin_amdgcn_s_setprio(FPRIO_C);
    if (mode != 0) {
#pragma unroll
      for (int m = 0; m < 8; ++m) yin[m] = mode == 2 ? yin[m] >> 16 : yin[m] & 0xffffu;
    }
    cf v[8];
#pragma unroll
    for (int m = 0; m < 8; ++m) {
      const h16x2 hv = __builtin_bit_cast(h16x2, yin[m]);
      v[m] = cf{(float)hv[0], (float)hv[1]};
    }
    dft4<-1>(v[0], v[2], v[4], v[6]);
    dft4<-1>(v[1], v[3], v[5], v[7]);
#pragma unroll
    for (int q = 0; q < 4; ++q) {
      lds_wc(L, e1 + 4096 * q, v[2 * q]);
      lds_wc(L, e1 + 4096 * q + 2048, v[2 * q + 1]);
    }
    grp_sync(L, L_CTR + 8, 4u);
#pragma unroll
    for (int r = 0; r < 8; ++r) v[r] = ctw<-1>(lds_rc(L, FA.rd[r & 1] + 512 * r), t1[r]);
    FENCE();
    fft512<-1>(v, tb, tc, L, FA);
    SCHED();
    wait_vm<0>();
    asm volatile("" : "+v"(wq[0]), "+v"(wq[1]));
#pragma unroll
    for (int r = 0; r < 8; ++r) {
      const unsigned u = wq[r >> 2][r & 3];
      v[r] = ctw<1>(v[r], cf{bf_lo(u), bf_hi(u)});
    }
    fft512<1>(v, tb, tc, L, FA);
#pragma unroll
    for (int r = 0; r < 8; ++r) lds_wc(L, FA.rd[r & 1] + 512 * r, ctw<1>(v[r], t1[r]));
    grp_sync(L, L_CTR + 8, 8u);
#pragma unroll
    for (int q = 0; q < 4; ++q) {
      v[2 * q] = lds_rc(L, e2a + 4096 * q);
      v[2 * q + 1] = lds_rc(L, e2b + 4096 * q);
    }
    dft4<1>(v[0], v[2], v[4], v[6]);
    dft4<1>(v[1], v[3], v[5], v[7]);
    __builtin_amdgcn_s_setprio(FPRIO);
    {
      const unsigned so = OFF_ZT + (unsigned)slot * 8192u;
      if (mode == 0) {
        st8_sc1<0>(v2u{pk_h(v[0].x, v[0].y), pk_h(v[1].x, v[1].y)}, wsr, tp * 8u, so);
        st8_sc1<2048>(v2u{pk_h(v[2].x, v[2].y), pk_h(v[3].x, v[3].y)}, wsr, tp * 8u, so);
        st8_sc1<0>(v2u{pk_h(v[4].x, v[4].y), pk_h(v[5].x, v[5].y)}, wsr, tp * 8u, so + 4096);
        st8_sc1<2048>(v2u{pk_h(v[6].x, v[6].y), pk_h(v[7].x, v[7].y)}, wsr, tp * 8u, so + 4096);
      } else {
        const unsigned vo = tp * 8u + (mode == 2 ? 2u : 0u);
        st2_sc1<0>(pk_h(v[0].x, 0.f), wsr, vo, so); st2_sc1<4>(pk_h(v[1].x, 0.f), wsr, vo, so);
        st2_sc1<2048>(pk_h(v[2].x, 0.f), wsr, vo, so); st2_sc1<2052>(pk_h(v[3].x, 0.f), wsr, vo, so);
        st2_sc1<0>(pk_h(v[4].x, 0.f), wsr, vo, so + 4096); st2_sc1<4>(pk_h(v[5].x, 0.f), wsr, vo, so + 4096);
        st2_sc1<2048>(pk_h(v[6].x, 0.f), wsr, vo, so + 4096); st2_sc1<2052>(pk_h(v[7].x, 0.f), wsr, vo, so + 4096);
      }
    }
    STAMP(11);
    const unsigned g4 = lane >> 4, n16 = lane & 15;
    v4u gf[4][4];
    auto make_g = [&](int i) __attribute__((always_inline)) {
      int u = 4 * i + fw;
      u = u < 14 ? u : 13;
      const int ks = u < 2 ? u + 12 : u - 2;
#pragma unroll
      for (int d4 = 0; d4 < 4; ++d4) {
        const unsigned dcol = 64 * dt + 16 * ((d4 + fw) & 3) + n16;
        const float df = (float)dcol, fa = rev448(df, (float)(16 * ks + 4 * g4)), fs = df * (1.0f / 448.0f);
#pragma unroll
        for (int e = 0; e < 4; ++e) {
          const float a = __builtin_fmaf((float)e, fs, fa);
          gf[i][d4][e] = pk_h(__builtin_amdgcn_cosf(a), __builtin_amdgcn_sinf(-a));
        }
        if (i == 0) {
          if (fw == 2 && g4 == 0) gf[0][d4][0] = (n16 & 1) ? 0xb8003800u : 0x38003800u;
        }
        PIN(gf[i][d4]);
      }
    };
    __builtin_amdgcn_s_setprio(FPRIO_G);
    if (is_main) {
      finish_z1([](auto) {});
      grp_arrive(L, L_CTR + 0);
    }
    __builtin_amdgcn_s_setprio(FPRIO);
    wait_vm<0>();
    if (grp_arrive_last(L, L_CTR + 12, 4u)) {
      if (lane == 0) st4_sc1<0>(tag, wsr, OFF_FZ + (unsigned)kw * 16u, 0);
    }
    STAMP(12);
    if (!is_main) {
      STAMP_OUT();
      return;
    }
    __builtin_amdgcn_s_setprio(FPRIO_G);
    make_g(0);
    make_g(1);
    make_g(2);
    make_g(3);
    __builtin_amdgcn_s_setprio(FPRIO);
    grp_sync(L, L_CTR + 8, 12u);
    STAMP(13);
    __builtin_amdgcn_s_setprio(0);
    grp_wait_sleep(L, L_CTR + 24 + 4 * fw, 1u);
    __builtin_amdgcn_s_setprio(FPRIO);
    {
      const unsigned ug = 4 * g4 + fw;
      const unsigned kg = ug < 2 ? ug + 12 : ug - 2;
      const unsigned fo = ug < 14 ? kg * 256u + n16 * 16u : 224u * 16u;
      poll_flags<6>(wsr, OFF_FZ + fo, ug <= 14, tag, 5u);
    }
    STAMP(19);
    STAMPX(4, 1);
    v4u zr[4][4];
    {
      const unsigned vo = g4 * 32768u + n16 * 16u;
#pragma unroll
      for (int i = 0; i < 4; ++i) {
        int u = 4 * i + fw;
        u = u < 14 ? u : 13;
        const int ks = u < 2 ? u + 12 : u - 2;
        const unsigned so = OFF_ZT + (unsigned)ks * (16u * 8192u) + (unsigned)(128 * p + 64 * th) * 4u;
#pragma unroll
        for (int c = 0; c < 4; ++c) ld16_sc1<0>(zr[i][c], wsr, (i == 3 && fw >= 2) ? WS_BYTES : vo, so + c * 8192);
      }
      const unsigned rv = (unsigned)(128 * p + 64 * th + 16 * fw + (lane >> 3)) * 896u + (unsigned)dt * 128u + (lane & 7) * 16u;
      dma16x2(xr, rv, rv + 8u * 896u - 1024u, 0, lbase + L_XR + fw * 2048);
    }
    const v4u wor = make_rsrc(P.wout, 896u * 896u);
    const unsigned ov0 = (lane >> 3) * 896u + (unsigned)dt * 128u + (((lane & 7) ^ (((lane >> 4) & 1) << 2)) << 4);
    const unsigned ov1 = ov0 + 8u * 896u - 1024u, ov2 = ov0 + 16u * 896u - 2048u, ov3 = ov0 + 24u * 896u - 3072u;
    auto issue_wo = [&](int i) __attribute__((always_inline)) { dma16x4(wor, ov0, ov1, ov2, ov3, (unsigned)((7 * fw + i) * 32) * 896u, lbase + ring_base(i) + fw * 4096); };
    __builtin_amdgcn_s_setprio(FPRIO_A);
    grp_wait(L, L_CTR + 24 + 4 * fw, 2u);
    STAMPX(4, 2);
    f32x4 sacc[4][4];
#pragma unroll
    for (int a = 0; a < 4; ++a)
#pragma unroll
      for (int b = 0; b < 4; ++b) sacc[a][b] = f32x4{0.f, 0.f, 0.f, 0.f};
    static_for<0, 4>([&](auto ic) __attribute__((always_inline)) {
      constexpr int i = decltype(ic)::value, sl = (4 + i) % 7;
      v4u(&z4)[4] = zr[i];
      asm volatile("s_waitcnt vmcnt(14)" : "+v"(z4[0]), "+v"(z4[1]), "+v"(z4[2]), "+v"(z4[3])::"memory");
      set_m0(lbase + ring_base(sl) + fw * 4096);
      static_for<0, 4>([&](auto tc) __attribute__((always_inline)) {
        constexpr int ti = decltype(tc)::value;
        const v4u zf = v4u{z4[0][ti], z4[1][ti], z4[2][ti], z4[3][ti]};
#pragma unroll
        for (int d4 = 0; d4 < 4; ++d4) sacc[ti][d4] = mfma_h(gf[i][d4], zf, sacc[ti][d4]);
        SCHED();
        dma_piece<ti * 1024>(wor, ti == 0 ? ov0 : ti == 1 ? ov1 : ti == 2 ? ov2 : ov3, (unsigned)((7 * fw + sl) * 32) * 896u);
        SCHED();
      });
    });
#pragma unroll
    for (int r = 0; r < 4; ++r) {
      const unsigned sb = L_FFT + 4096u * ((fw + r) & 3) + lane * 16;
      STAMPX(4, 3 + 2 * r);
      if (r > 0) {
        grp_wait(L, L_CTR + 8, 12u + 4u * r);
        f32x4 t[4];
#pragma unroll
        for (int ti = 0; ti < 4; ++ti) t[ti] = *(LAS f32x4*)(L + sb + ti * 1024);
        SCHED();
#pragma unroll
        for (int ti = 0; ti < 4; ++ti) sacc[ti][r] += t[ti];
      }
#pragma unroll
      for (int ti = 0; ti < 4; ++ti) *(LAS f32x4*)(L + sb + ti * 1024) = sacc[ti][r];
      if (r < 3) grp_arrive(L, L_CTR + 8);
      STAMPX(4, 4 + 2 * r);
      if (r == WOR) {
        grp_wait(L, L_CTR + 20, 4u);
        issue_wo(1); issue_wo(2); issue_wo(3);
      }
    }
    __builtin_amdgcn_s_setprio(0);
    STAMPX(1, 9);
  }

  RELANE();
  const int tp2 = w & 1, kq = w >> 1;
  unsigned ob[2][2];
#pragma unroll
  for (int d2 = 0; d2 < 2; ++d2) {
    ob[d2][0] = kq * 4096 + (4 * h2 + trq) * 128 + ((d2 ^ (trq >> 1)) << 6) + 32 * trb + 8 * trp;
    ob[d2][1] = ob[d2][0] + RING_HI;
    PIN(ob[d2][0]); PIN(ob[d2][1]);
  }
  unsigned fho = OFF_FH + (unsigned)(p * 4 + kq) * 128u + (lane & 3) * 32u;
  PIN(fho);
  unsigned da[4], c0, c1, oo, sb, xb;
  {
    const unsigned rb = 16384u * (2 * tp2 + (r32 >> 4)) + kq * 4096 + (r32 & 15) * 256;
#pragma unroll
    for (int g = 0; g < 4; ++g) { da[g] = rb + (((2 * g + h2) ^ (r32 & 7)) << 4); PIN(da[g]); }
    const unsigned t = tid >> 3, c8 = tid & 7, tr = t & 15;
    sb = L_FFT + (c8 >> 1) * 4096 + (t & 3) * 1024 + (c8 & 1) * 512 + (t >> 2) * 16;
    xb = L_XR + tid * 16;
    c0 = 16384u * (w >> 1) + tr * 256 + (((2 * c8) ^ (tr & 7)) << 4);
    c1 = c0 ^ 16u;
    oo = (128 * p + 64 * th + t) * 896 + dt * 128 + c8 * 16;
    PIN(c0); PIN(c1); PIN(oo); PIN(sb); PIN(xb);
  }
  f32x4 s0, s1;
  float r[8];
  wait_vm<0>();
  bar();
  STAMP(15);
  STAMPX(4, 11);
  f32x16 oa[2] = {splat16(0.f), splat16(0.f)};
  {
    v4u wo[14][2];
    auto rd_wo = [&](int i) __attribute__((always_inline)) {
#pragma unroll
      for (int d2 = 0; d2 < 2; ++d2) {
        const unsigned rb = ring_base(i >> 1);
        const unsigned a0 = (rb >= RING_HI ? ob[d2][1] + (rb - RING_HI) : ob[d2][0] + rb) + (i & 1) * 2048;
        wo[i][d2] = tr_frag(L, a0, a0 + 1024);
      }
    };
#define WOA 5
    unsigned fhv;
    ld4_sc1<0>(fhv, wsr, fho, 0);
#pragma unroll
    for (int i = 0; i < WOA; ++i) rd_wo(i);
    SCHED();
    wait_vm<0>();
    PIN(fhv);
    if (!wave_all(fhv == tag)) poll_flags<HSL>(wsr, fho, true, tag, 6u);
    STAMP(16);
    v4u hf[14];
    const unsigned so = OFF_H + (unsigned)((p * 4 + 2 * th + tp2) * 56 + 14 * kq) * 1024u;
#define HLAG 10
    static_for<0, 14 + HLAG>([&](auto ic) __attribute__((always_inline)) {
      constexpr int i = decltype(ic)::value, m = i - HLAG;
      if constexpr (i < 14) ld16_sc1<(i & 3) * 1024>(hf[i], wsr, lane * 16u, so + (i >> 2) * 4096);
      SCHED();
      if constexpr (WOA + i < 14) rd_wo(WOA + i);
      if constexpr (WOA + i == 13) grp_arrive(L, L_CTR + 40);
      SCHED();
      if constexpr (m >= 0) {
        asm volatile("s_waitcnt vmcnt(%1)" : "+v"(hf[m]) : "i"((i < 14 ? i : 13) - m) : "memory");
        oa[0] = mfma32(wo[m][0], hf[m], oa[0]);
        oa[1] = mfma32(wo[m][1], hf[m], oa[1]);
        SCHED();
        if constexpr (m == 11) grp_wait(L, L_CTR + 40, 8u);
      }
      SCHED();
    });
  }
  STAMP(17);
#pragma unroll
  for (int d2 = 0; d2 < 2; ++d2)
#pragma unroll
    for (int g = 0; g < 4; ++g) *(LAS f32x4*)(L + da[g] + d2 * 128) = f32x4{oa[d2][4 * g], oa[d2][4 * g + 1], oa[d2][4 * g + 2], oa[d2][4 * g + 3]};
  {
    s0 = *(LAS f32x4*)(L + sb); s1 = *(LAS f32x4*)(L + sb + 256);
    const v4u xq = *(LAS v4u*)(L + xb);
    bar();
    f32x4 o0[4], o1[4];
#pragma unroll
    for (int q = 0; q < 4; ++q) {
      o0[q] = *(LAS f32x4*)(L + q * 4096 + c0);
      o1[q] = *(LAS f32x4*)(L + q * 4096 + c1);
    }
    SCHED();
    float o[8], u[8];
#pragma unroll
    for (int e = 0; e < 4; ++e) {
      o[e] = (o0[0][e] + o0[1][e]) + (o0[2][e] + o0[3][e]);
      o[4 + e] = (o1[0][e] + o1[1][e]) + (o1[2][e] + o1[3][e]);
    }
#pragma unroll
    for (int e = 0; e < 8; ++e) u[e] = o[e] * -1.442695041f;
    SCHED();
#pragma unroll
    for (int e = 0; e < 8; ++e) u[e] = __builtin_amdgcn_exp2f(u[e]);
#pragma unroll
    for (int e = 0; e < 8; ++e) o[e] *= e < 4 ? s0[e & 3] : s1[e & 3];
    SCHED();
#pragma unroll
    for (int e = 0; e < 8; ++e) u[e] = __builtin_fmaf(u[e], 224.0f, 224.0f);
    SCHED();
#pragma unroll
    for (int e = 0; e < 8; ++e) u[e] = __builtin_amdgcn_rcpf(u[e]);
#pragma unroll
    for (int e = 0; e < 8; ++e) r[e] = (e & 1) ? bf_hi(xq[e >> 1]) : bf_lo(xq[e >> 1]);
    SCHED();
#pragma unroll
    for (int e = 0; e < 8; ++e) r[e] = __builtin_fmaf(o[e], u[e], r[e]);
    const v4u res = v4u{pk_bf(r[0], r[1]), pk_bf(r[2], r[3]), pk_bf(r[4], r[5]), pk_bf(r[6], r[7])};
    *(GAS v4u*)((GAS unsigned char*)P.out + oo) = res;
  }
  if (bid == 0 && tid == 0) st4_sc1<0>(tag, wsr, OFF_EPOCH, 0);
  STAMP(18);
  STAMP_OUT();
}
__device__ __attribute__((used, noinline)) void code_end_marker() { asm volatile("code_end_lbl:"); }

extern "C" void kernel_launch(void* const* d_in, const int* in_sizes, int n_in, void* d_out, int out_size, void* d_ws, size_t ws_size,
                              hipStream_t stream) {
  Params P;
  P.x = (const unsigned short*)d_in[0];
  P.wr = (const unsigned short*)d_in[1];
  P.wi = (const unsigned short*)d_in[2];
  P.win = (const unsigned short*)d_in[4];
  P.wsp = (const unsigned short*)d_in[5];
  P.bsp = (const unsigned short*)d_in[6];
  P.wout = (const unsigned short*)d_in[7];
  P.out = (unsigned short*)d_out;
  P.ws = (unsigned char*)d_ws;
  fused_kernel<<<256, 512, 0, stream>>>(P);
}
```

```cpp
#define LATE0 4
#include <hip/hip_runtime.h>
#include <stdint.h>

typedef unsigned v4u __attribute__((ext_vector_type(4)));
typedef unsigned v2u __attribute__((ext_vector_type(2)));
typedef float f32x4 __attribute__((ext_vector_type(4)));
typedef float f32x2 __attribute__((ext_vector_type(2)));
typedef float f32x16 __attribute__((ext_vector_type(16)));
typedef short s16x8 __attribute__((ext_vector_type(8)));
typedef short s16x4 __attribute__((ext_vector_type(4)));
typedef _Float16 h16x8 __attribute__((ext_vector_type(8)));
typedef _Float16 h16x2 __attribute__((ext_vector_type(2)));
typedef __bf16 b16x2 __attribute__((ext_vector_type(2)));
#define LAS __attribute__((address_space(3)))
#define GAS __attribute__((address_space(1)))
#define CAS __attribute__((address_space(4)))
#define DEVINL __device__ __forceinline__
#if defined(__HIP_DEVICE_COMPILE__)
#define NO_PK __attribute__((target("no-packed-fp32-ops")))
#else
#define NO_PK
#endif

#define STAMPS 0

constexpr unsigned OFF_EPOCH = 0, OFF_DBG = 128;
constexpr unsigned OFF_FA = 4096;
constexpr unsigned OFF_FY = 12288;
constexpr unsigned OFF_FW = 20480;
constexpr unsigned OFF_FZ = 24576;
constexpr unsigned OFF_FH = 32768;
constexpr unsigned OFF_ST = 65536;
constexpr unsigned OFF_Z2N = 1u << 20;
constexpr unsigned OFF_YT = 5u << 20;
constexpr unsigned OFF_WT = 7u << 20;
constexpr unsigned OFF_ZT = 9u << 20;
constexpr unsigned OFF_H = 11u << 20;
constexpr unsigned OFF_STAMP = 16u << 20;
constexpr unsigned WS_BYTES = 20u << 20;

constexpr unsigned L_F = 65536;
constexpr unsigned L_W2 = 122880;
constexpr unsigned L_FFT = 114688;
constexpr unsigned L_Z1 = 131072;
constexpr unsigned L_XR = 147456;
constexpr unsigned L_CS2 = 155648;
constexpr unsigned L_CS1 = L_CS2 + 1024;
constexpr unsigned L_XS = L_CS1 + 1024;
constexpr unsigned L_CTR = L_XS + 1024;
constexpr unsigned L_BSP = L_CTR + 256;
constexpr unsigned L_ZS = L_CTR + 1024;
constexpr unsigned L_PS = L_ZS + 1024;
constexpr unsigned LDS_BYTES = 163840;
__host__ __device__ __forceinline__ constexpr unsigned w1_base(int i) { return i < 2 ? 49152u + 8192u * i : 8192u * (i - 2); }
__host__ __device__ __forceinline__ constexpr unsigned ring_base(int i) { return i < 3 ? 65536u + 16384u * i : 16384u * (i - 3); }
constexpr unsigned RING_HI = 65536;

#define GPRIO_ 1
#define GATH 6
#define STWARM 1
#define FLAGT 22
#define YLT 31
#define A3S 26
#define WOG 0
#define WOR 0
#define ZLATE 1
#define HSL 2
#define ASL 0
constexpr unsigned SPIN_CAP = 1u << 14;

struct Params {
  const unsigned short* x;
  const unsigned short* wr;
  const unsigned short* wi;
  const unsigned short* win;
  const unsigned short* wsp;
  const unsigned short* bsp;
  const unsigned short* wout;
  unsigned short* out;
  unsigned char* ws;
};

template <int V> struct IC { static constexpr int value = V; };
template <int I, int N, class F> DEVINL void static_for(F&& f) {
  if constexpr (I < N) {
    f(IC<I>{});
    static_for<I + 1, N>(f);
  }
}
DEVINL v4u make_rsrc(const void* p, unsigned bytes) {
  const unsigned long long a = (unsigned long long)p;
  v4u r;
  r[0] = __builtin_amdgcn_readfirstlane((unsigned)a);
  r[1] = __builtin_amdgcn_readfirstlane((unsigned)(a >> 32) & 0xffffu);
  r[2] = bytes;
  r[3] = 0x00020000u;
  return r;
}
DEVINL void dma16(v4u rs, unsigned voff, unsigned soff, unsigned lds) {
  asm volatile("s_mov_b32 m0, %3\n\ts_nop 0\n\tbuffer_load_dwordx4 %0, %1, %2 offen lds" ::"v"(voff), "s"(rs), "s"(soff), "s"(lds) : "memory");
}
DEVINL void dma16x2(v4u rs, unsigned v0, unsigned v1, unsigned soff, unsigned lds) {
  asm volatile("s_mov_b32 m0, %4\n\ts_nop 0\n\tbuffer_load_dwordx4 %0, %2, %3 offen lds\n\tbuffer_load_dwordx4 %1, %2, %3 offen offset:1024 lds" ::"v"(v0),
               "v"(v1), "s"(rs), "s"(soff), "s"(lds)
               : "memory");
}
DEVINL void dma16x4(v4u rs, unsigned v0, unsigned v1, unsigned v2, unsigned v3, unsigned soff, unsigned lds) {
  asm volatile(
      "s_mov_b32 m0, %6\n\ts_nop 0\n\tbuffer_load_dwordx4 %0, %4, %5 offen lds\n\tbuffer_load_dwordx4 %1, %4, %5 offen offset:1024 lds\n\t"
      "buffer_load_dwordx4 %2, %4, %5 offen offset:2048 lds\n\tbuffer_load_dwordx4 %3, %4, %5 offen offset:3072 lds" ::"v"(v0),
      "v"(v1), "v"(v2), "v"(v3), "s"(rs), "s"(soff), "s"(lds)
      : "memory");
}
DEVINL void set_m0(unsigned lds) { asm volatile("s_mov_b32 m0, %0\n\ts_nop 0" ::"s"(lds) : "memory"); }
template <int IMM> DEVINL void dma_piece(v4u rs, unsigned voff, unsigned soff) {
  asm volatile("buffer_load_dwordx4 %0, %1, %2 offen offset:%3 lds" ::"v"(voff), "s"(rs), "s"(soff), "i"(IMM) : "memory");
}
DEVINL void dma4(v4u rs, unsigned voff, unsigned soff, unsigned lds) {
  asm volatile("s_mov_b32 m0, %3\n\ts_nop 0\n\tbuffer_load_dword %0, %1, %2 offen lds" ::"v"(voff), "s"(rs), "s"(soff), "s"(lds) : "memory");
}
template <int IMM> DEVINL void ld16_sc1(v4u& d, v4u rs, unsigned voff, unsigned soff) {
  asm volatile("buffer_load_dwordx4 %0, %1, %2, %3 offen offset:%4 sc1" : "=v"(d) : "v"(voff), "s"(rs), "s"(soff), "i"(IMM) : "memory");
}
template <int IMM> DEVINL void ld4_sc1(unsigned& d, v4u rs, unsigned voff, unsigned soff) {
  asm volatile("buffer_load_dword %0, %1, %2, %3 offen offset:%4 sc1" : "=v"(d) : "v"(voff), "s"(rs), "s"(soff), "i"(IMM) : "memory");
}
template <int IMM> DEVINL void st16_sc1(v4u d, v4u rs, unsigned voff, unsigned soff) {
  asm volatile("buffer_store_dwordx4 %0, %1, %2, %3 offen offset:%4 sc1\n\ts_nop 1" ::"v"(d), "v"(voff), "s"(rs), "s"(soff), "i"(IMM) : "memory");
}
template <int IMM> DEVINL void st8_sc1(v2u d, v4u rs, unsigned voff, unsigned soff) {
  asm volatile("buffer_store_dwordx2 %0, %1, %2, %3 offen offset:%4 sc1" ::"v"(d), "v"(voff), "s"(rs), "s"(soff), "i"(IMM) : "memory");
}
template <int IMM> DEVINL void st4_sc1(unsigned d, v4u rs, unsigned voff, unsigned soff) {
  asm volatile("buffer_store_dword %0, %1, %2, %3 offen offset:%4 sc1" ::"v"(d), "v"(voff), "s"(rs), "s"(soff), "i"(IMM) : "memory");
}
template <int IMM> DEVINL void st2_sc1(unsigned d, v4u rs, unsigned voff, unsigned soff) {
  asm volatile("buffer_store_short %0, %1, %2, %3 offen offset:%4 sc1" ::"v"(d), "v"(voff), "s"(rs), "s"(soff), "i"(IMM) : "memory");
}
template <int N> DEVINL void wait_vm() { asm volatile("s_waitcnt vmcnt(%0)" ::"i"(N) : "memory"); }
DEVINL void bar() {
  asm volatile("s_waitcnt lgkmcnt(0)" ::: "memory");
  __builtin_amdgcn_s_barrier();
  asm volatile("" ::: "memory");
}
#define SCHED() __builtin_amdgcn_sched_barrier(0)
#define PIN(x) asm volatile("" : "+v"(x))
#define FENCE() asm volatile("" ::: "memory")

DEVINL f32x16 mfma32(v4u a, v4u b, f32x16 c) {
  return __builtin_amdgcn_mfma_f32_32x32x16_bf16(__builtin_bit_cast(s16x8, a), __builtin_bit_cast(s16x8, b), c, 0, 0, 0);
}
DEVINL f32x4 mfma16(v4u a, v4u b, f32x4 c) {
  return __builtin_amdgcn_mfma_f32_16x16x32_bf16(__builtin_bit_cast(s16x8, a), __builtin_bit_cast(s16x8, b), c, 0, 0, 0);
}
DEVINL f32x4 mfma_h(v4u a, v4u b, f32x4 c) {
  return __builtin_amdgcn_mfma_f32_16x16x32_f16(__builtin_bit_cast(h16x8, a), __builtin_bit_cast(h16x8, b), c, 0, 0, 0);
}
DEVINL f32x16 splat16(float v) { return f32x16{v, v, v, v, v, v, v, v, v, v, v, v, v, v, v, v}; }
DEVINL unsigned pk_bf(float a, float b) { return __builtin_bit_cast(unsigned, __builtin_convertvector((f32x2{a, b}), b16x2)); }
DEVINL unsigned pk_h(float a, float b) { return __builtin_bit_cast(unsigned, __builtin_convertvector((f32x2{a, b}), h16x2)); }
DEVINL float asf(unsigned u) { return __builtin_bit_cast(float, u); }
DEVINL unsigned asu(float f) { return __builtin_bit_cast(unsigned, f); }
DEVINL float bf_lo(unsigned u) { return __builtin_bit_cast(float, u << 16); }
DEVINL float bf_hi(unsigned u) { return __builtin_bit_cast(float, u & 0xffff0000u); }
template <class H> DEVINL void gelu16(f32x16& t, float k1, float k2, float sg, H&& hook) {
  float u[16];
#pragma unroll
  for (int e = 0; e < 16; ++e) u[e] = t[e] * t[e];
#pragma unroll
  for (int e = 0; e < 16; ++e) u[e] = __builtin_fmaf(u[e], k2, k1);
  SCHED();
#pragma unroll
  for (int e = 0; e < 16; ++e) u[e] = u[e] * t[e];
  SCHED();
  hook(IC<0>{});
  SCHED();
#pragma unroll
  for (int e = 0; e < 16; ++e) u[e] = __builtin_amdgcn_exp2f(u[e]);
  SCHED();
#pragma unroll
  for (int e = 0; e < 16; ++e) u[e] = __builtin_fmaf(u[e], sg, sg);
  SCHED();
  hook(IC<1>{});
  SCHED();
#pragma unroll
  for (int e = 0; e < 16; ++e) u[e] = __builtin_amdgcn_rcpf(u[e]);
  SCHED();
#pragma unroll
  for (int e = 0; e < 16; ++e) t[e] = t[e] * u[e];
}
DEVINL v4u tr_frag(LAS unsigned char* L, unsigned a0, unsigned a1) {
  const s16x4 lo = __builtin_amdgcn_ds_read_tr16_b64_v4i16((LAS s16x4*)(L + a0));
  const s16x4 hi = __builtin_amdgcn_ds_read_tr16_b64_v4i16((LAS s16x4*)(L + a1));
  const v2u l2 = __builtin_bit_cast(v2u, lo), h2 = __builtin_bit_cast(v2u, hi);
  return v4u{l2[0], l2[1], h2[0], h2[1]};
}
DEVINL bool wave_all(bool c) { return __builtin_amdgcn_ballot_w64(!c) == 0ull; }
DEVINL float half_sum(float v) {
  const unsigned u = asu(v);
  const auto r = __builtin_amdgcn_permlane32_swap(u, u, false, false);
  const unsigned lo = r[0], hi = r[1];
  return asf(lo) + asf(hi);
}
DEVINL float row_pair_sum(float v) {
  const unsigned u = asu(v);
  const auto r = __builtin_amdgcn_permlane16_swap(u, u, false, false);
  const unsigned lo = r[0], hi = r[1];
  return asf(lo) + asf(hi);
}
DEVINL unsigned fresh_lane() {
  unsigned l;
  asm volatile("v_mbcnt_lo_u32_b32 %0, -1, 0\n\tv_mbcnt_hi_u32_b32 %0, -1, %0" : "=v"(l));
  return l;
}
DEVINL float rev448(float a, float b) {
  const float pr = a * b;
  const float q = __builtin_floorf(pr * (1.0f / 448.0f));
  return __builtin_fmaf(q, -448.0f, pr) * (1.0f / 448.0f);
}

template <int SLEEP> DEVINL void poll_flags(v4u wsr, unsigned voff, bool active, unsigned tag, unsigned code) {
#pragma unroll 1
  for (unsigned spins = 0;; ++spins) {
    unsigned f;
    asm volatile("buffer_load_dword %0, %1, %2, 0 offen sc1\n\ts_waitcnt vmcnt(0)" : "=v"(f) : "v"(voff), "s"(wsr) : "memory");
    if (wave_all(!active || f == tag)) break;
    if (spins > SPIN_CAP) {
      st4_sc1<0>(code, wsr, OFF_DBG, 0);
      break;
    }
    if (SLEEP) __builtin_amdgcn_s_sleep(SLEEP);
  }
}
DEVINL void grp_arrive(LAS unsigned char* L, unsigned ctr) {
  const unsigned a = (unsigned)(unsigned long long)L + ctr;
  asm volatile("s_mov_b64 exec, 1\n\tds_add_u32 %0, %1\n\ts_mov_b64 exec, -1" ::"v"(a), "v"(1u) : "memory");
}
DEVINL void grp_wait(LAS unsigned char* L, unsigned ctr, unsigned count) {
  const unsigned a = (unsigned)(unsigned long long)L + ctr;
#pragma unroll 1
  for (unsigned spins = 0; spins < (1u << 22); ++spins) {
    unsigned v;
    asm volatile("ds_read_b32 %0, %1\n\ts_waitcnt lgkmcnt(0)" : "=v"(v) : "v"(a) : "memory");
    if ((unsigned)__builtin_amdgcn_readfirstlane(v) >= count) break;
  }
}
DEVINL void grp_wait_sleep(LAS unsigned char* L, unsigned ctr, unsigned count) {
  const unsigned a = (unsigned)(unsigned long long)L + ctr;
#pragma unroll 1
  for (unsigned spins = 0; spins < (1u << 20); ++spins) {
    unsigned v;
    asm volatile("ds_read_b32 %0, %1\n\ts_waitcnt lgkmcnt(0)" : "=v"(v) : "v"(a) : "memory");
    if ((unsigned)__builtin_amdgcn_readfirstlane(v) >= count) break;
    __builtin_amdgcn_s_sleep(2);
  }
}
DEVINL void grp_sync(LAS unsigned char* L, unsigned ctr, unsigned count) {
  grp_arrive(L, ctr);
  grp_wait(L, ctr, count);
}
DEVINL bool grp_arrive_last(LAS unsigned char* L, unsigned ctr, unsigned total) {
  const unsigned a = (unsigned)(unsigned long long)L + ctr;
  unsigned v = 1u;
  asm volatile("s_mov_b64 exec, 1\n\tds_add_rtn_u32 %0, %1, %0\n\ts_waitcnt lgkmcnt(0)\n\ts_mov_b64 exec, -1" : "+v"(v) : "v"(a) : "memory");
  return (unsigned)__builtin_amdgcn_readfirstlane(v) == total - 1;
}

#define STAMP(i) do {} while (0)
#define STAMPX(set, i) do {} while (0)
#define STAMP_OUT() do {} while (0)

struct cf { float x, y; };
DEVINL cf cadd(cf a, cf b) { return cf{a.x + b.x, a.y + b.y}; }
DEVINL cf csub(cf a, cf b) { return cf{a.x - b.x, a.y - b.y}; }
template <int SG> DEVINL cf rot90(cf a) { return SG < 0 ? cf{a.y, -a.x} : cf{-a.y, a.x}; }
template <int SG> DEVINL cf ctw(cf a, cf w) {
  return SG < 0 ? cf{a.x * w.x + a.y * w.y, a.y * w.x - a.x * w.y} : cf{a.x * w.x - a.y * w.y, a.y * w.x + a.x * w.y};
}
template <int SG> DEVINL void dft4(cf& a0, cf& a1, cf& a2, cf& a3) {
  const cf s0 = cadd(a0, a2), d0 = csub(a0, a2), s1 = cadd(a1, a3), d1 = rot90<SG>(csub(a1, a3));
  a0 = cadd(s0, s1); a2 = csub(s0, s1); a1 = cadd(d0, d1); a3 = csub(d0, d1);
}
template <int SG> DEVINL void dft8(cf (&a)[8]) {
  cf e0 = a[0], e1 = a[2], e2 = a[4], e3 = a[6], o0 = a[1], o1 = a[3], o2 = a[5], o3 = a[7];
  dft4<SG>(e0, e1, e2, e3);
  dft4<SG>(o0, o1, o2, o3);
  const float h = 0.70710678118654752f;
  const cf r1 = rot90<SG>(o1), r3 = rot90<SG>(o3);
  o1 = cf{o1.x + r1.x, o1.y + r1.y};
  o2 = rot90<SG>(o2);
  o3 = cf{r3.x - o3.x, r3.y - o3.y};
  a[0] = cadd(e0, o0); a[4] = csub(e0, o0);
  a[1] = cf{__builtin_fmaf(o1.x, h, e1.x), __builtin_fmaf(o1.y, h, e1.y)}; a[5] = cf{__builtin_fmaf(o1.x, -h, e1.x), __builtin_fmaf(o1.y, -h, e1.y)};
  a[2] = cadd(e2, o2); a[6] = csub(e2, o2);
  a[3] = cf{__builtin_fmaf(o3.x, h, e3.x), __builtin_fmaf(o3.y, h, e3.y)}; a[7] = cf{__builtin_fmaf(o3.x, -h, e3.x), __builtin_fmaf(o3.y, -h, e3.y)};
}
DEVINL unsigned fsw(unsigned i) { return i ^ (((i >> 4) & 7u) | (((i >> 6) & 1u) << 3)); }
DEVINL void lds_wc(LAS unsigned char* L, unsigned a, cf v) { *(LAS f32x2*)(L + a) = f32x2{v.x, v.y}; }
DEVINL cf lds_rc(LAS unsigned char* L, unsigned a) { const f32x2 v = *(LAS f32x2*)(L + a); return cf{v[0], v[1]}; }
struct FftAddr { unsigned rd[2], wa[8], wb[8]; };
template <int SG> DEVINL void fft512(cf (&v)[8], const cf (&tb)[7], const cf (&tc)[7], LAS unsigned char* L, const FftAddr& A) {
  dft8<SG>(v);
#pragma unroll
  for (int r = 0; r < 8; ++r) lds_wc(L, A.wa[r], v[r]);
  FENCE();
#pragma unroll
  for (int r = 0; r < 8; ++r) v[r] = lds_rc(L, A.rd[r & 1] + 512 * r);
  FENCE();
#pragma unroll
  for (int r = 1; r < 8; ++r) v[r] = ctw<SG>(v[r], tb[r - 1]);
  dft8<SG>(v);
#pragma unroll
  for (int r = 0; r < 8; ++r) lds_wc(L, A.wb[r], v[r]);
  FENCE();
#pragma unroll
  for (int r = 0; r < 8; ++r) v[r] = lds_rc(L, A.rd[r & 1] + 512 * r);
  FENCE();
#pragma unroll
  for (int r = 1; r < 8; ++r) v[r] = ctw<SG>(v[r], tc[r - 1]);
  dft8<SG>(v);
}

constexpr int p2_dma_in(int s) { return (s >= 1 && s + 6 < 32) ? 4 : 0; }
#define GPRE 1
#define NPRE 7
#define BS 4
constexpr int p2_b_in(int s) { return (s >= 5 && ((s - 1) & 3) < 8 / BS && ((s - 1) >> 2) + 1 < 8) ? BS : 0; }
constexpr int p2_pos(int step) {
  int pos = 28 + 16;
  for (int s = 0; s < step; ++s) pos += p2_dma_in(s) + p2_b_in(s);
  return pos;
}
constexpr int p2_dma_done(int u) { return u < NPRE ? 4 * (u + 1) : u < 7 ? 4 * (u + 1) + 16 : p2_pos(u - 5) - p2_b_in(u - 6) / 4; }
constexpr int p2_b_done(int c) { return c < 2 ? 4 * NPRE + 8 * (c + 1) : p2_pos(4 * (c - 1) + 1 + 8 / BS); }

__global__ __launch_bounds__(512, 2) NO_PK void fused_kernel(Params P) {
  __shared__ __attribute__((aligned(16))) unsigned char smem[LDS_BYTES];
  LAS unsigned char* L = (LAS unsigned char*)smem;
  const unsigned lbase = (unsigned)(unsigned long long)L;
  int tid = __builtin_amdgcn_workitem_id_x();
  unsigned lane = tid & 63;
  const int w = __builtin_amdgcn_readfirstlane(tid >> 6);
  STAMP(0);
  unsigned pfd;
  {
    unsigned long long pc;
    unsigned csz;
    asm volatile("s_getpc_b64 %0\ncode_begin_lbl:\n\ts_mov_b32 %1, (code_end_lbl - code_begin_lbl)" : "=s"(pc), "=s"(csz));
    const unsigned o = (unsigned)tid * 128u;
#define PFALL 0
    const unsigned oc = (o < csz && (PFALL || __builtin_amdgcn_workgroup_id_x() >= 224)) ? o : 0u;
    asm volatile("global_load_dword %0, %1, %2" : "=v"(pfd) : "v"(oc), "s"(pc) : "memory");
  }
  const int bid = __builtin_amdgcn_workgroup_id_x();
  const int xcd = bid & 7, rr = bid >> 3;
  const bool is_main = rr < 28;
  const int p = 4 * (xcd >> 1) + (rr & 3);
  const int j = 7 * (xcd & 1) + (rr >> 2);
  const int sidx = xcd * 4 + (rr - 28);
  const v4u wsr = make_rsrc(P.ws, WS_BYTES);
  unsigned tag;
  unsigned h2 = lane >> 5, r32 = lane & 31;
  unsigned trq = (lane & 15) >> 2, trp = lane & 3, trb = (lane >> 4) & 1;
#define RELANE() do { lane = fresh_lane(); tid = w * 64 + lane; h2 = lane >> 5; r32 = lane & 31; trq = (lane & 15) >> 2; trp = lane & 3; trb = (lane >> 4) & 1; } while (0)
#define GET_TAG(ev) do { unsigned e_; asm volatile("v_readlane_b32 %0, %1, 63" : "=s"(e_) : "v"(ev), "v"(pfd) : "memory"); tag = e_ * 1664525u + 1013904223u; } while (0)

  f32x16 a1 = splat16(0.f);
  float gk1 = 0.f, gk2 = 0.f, gsg = 0.f;
  auto finish_z1 = [&](auto&& hook) __attribute__((always_inline)) {
    gelu16(a1, gk1, gk2, gsg, hook);
    v4u o[2];
#pragma unroll
    for (int e = 0; e < 8; ++e) o[e >> 2][e & 3] = pk_bf(a1[2 * e], a1[2 * e + 1]);
    *(LAS v4u*)(L + L_Z1 + w * 2048 + lane * 16) = o[0];
    *(LAS v4u*)(L + L_Z1 + w * 2048 + 1024 + lane * 16) = o[1];
  };
  unsigned yin[8];
  const int slot = is_main ? 16 * j + p : 0;
  const int kw = is_main ? slot : 224;
  const unsigned fft_flags = OFF_FY + (unsigned)(is_main ? j : 0) * 512u + (lane & 15) * 32u;
  auto load_fft_in = [&]() __attribute__((always_inline)) {
    const unsigned so = OFF_YT + (unsigned)slot * 8192u, vo = (tid - 256) * 4u;
    ld4_sc1<0>(yin[0], wsr, vo, so); ld4_sc1<1024>(yin[1], wsr, vo, so); ld4_sc1<2048>(yin[2], wsr, vo, so); ld4_sc1<3072>(yin[3], wsr, vo, so);
    ld4_sc1<0>(yin[4], wsr, vo, so + 4096); ld4_sc1<1024>(yin[5], wsr, vo, so + 4096); ld4_sc1<2048>(yin[6], wsr, vo, so + 4096); ld4_sc1<3072>(yin[7], wsr, vo, so + 4096);
  };
#define FFT_IN_LANDED() do { wait_vm<0>(); asm volatile("" : "+v"(yin[0]), "+v"(yin[1]), "+v"(yin[2]), "+v"(yin[3]), "+v"(yin[4]), "+v"(yin[5]), "+v"(yin[6]), "+v"(yin[7])::"memory"); } while (0)
  const int th = j / 7, dt = j % 7;
  const v4u xr = make_rsrc(P.x, 2048u * 896u);

  if (__builtin_expect(is_main, 1)) {
    const int wm = w & 3, wn = w >> 2;
    const v4u wir = make_rsrc(P.win, 448u * 3584u);
    const int c0 = (j + 2 * p) % 7;
    const unsigned ctab = (unsigned)(0x5432106543210ull >> (4 * c0));
    const unsigned xv0 = (unsigned)(128 * p + 16 * w + (lane >> 3)) * 896u + (((lane & 7) ^ (lane >> 4)) << 4);
    const unsigned xv1 = (xv0 ^ 64u) + 8u * 896u - 1024u;
    const unsigned wv = (unsigned)(8 * w + (lane >> 3)) * 3584u + (unsigned)j * 128u + (((lane & 7) ^ (((lane >> 4) & 1) << 2)) << 4);
    auto issue_x = [&](int i) __attribute__((always_inline)) { dma16x2(xr, xv0, xv1, ((ctab >> (4 * i)) & 7u) * 128u, lbase + (i & 3) * 16384 + w * 2048); };
    auto issue_w2 = [&](int i) __attribute__((always_inline)) { dma16(wir, wv, ((ctab >> (4 * i)) & 7u) * (64u * 3584u) + 1792u, lbase + L_W2 + (i & 3) * 8192 + w * 1024); };
    auto issue_w1 = [&](int i) __attribute__((always_inline)) { dma16(wir, wv, ((ctab >> (4 * i)) & 7u) * (64u * 3584u), lbase + w1_base(i) + w * 1024); };
    STAMPX(3, 1);
    issue_x(0); issue_w2(0);
    STAMPX(3, 2);
#define C1AT -1
#define C2AT 3
    if (C1AT < 0) { issue_x(1); issue_w2(1); }
    STAMPX(3, 3);
    if (tid < 16) *(LAS unsigned*)(L + L_CTR + 4 * tid) = 0u;

    {
      unsigned k = 16 * j + 8 * wn + ((lane & 15) >> 1);
      float ph = (lane & 1) ? 0.25f : 0.0f;
      if (k == 0 && (lane & 1)) { k = 224; ph = 0.0f; }
      const float kf = (float)k, fs = kf * (1.0f / 448.0f), dl = (float)(8 * (lane >> 4) + 32 * (wm & 1));
      const unsigned fo = L_F + (wn * 28 + 2 * wm + (lane >> 5)) * 1024 + (((lane >> 4) & 1) * 32 + (lane & 15)) * 16;
#pragma unroll
      for (int a = 0; a < 4; ++a) {
        if (a < 3 || wm < 2) {
          const unsigned cc = (ctab >> (4 * (2 * a + (wm >> 1)))) & 7u;
          const float fa = rev448(kf, dl + (float)(64 * cc)) + ph;
          float cv[8];
#pragma unroll
          for (int e = 0; e < 8; ++e) cv[e] = __builtin_amdgcn_cosf(__builtin_fmaf((float)e, fs, fa));
          SCHED();
          v4u fh, fl;
#pragma unroll
          for (int e = 0; e < 4; ++e) {
            const unsigned h0 = asu(cv[2 * e]) & 0xffff0000u, h1 = asu(cv[2 * e + 1]) & 0xffff0000u;
            fh[e] = __builtin_amdgcn_perm(h1, h0, 0x07060302u);
            fl[e] = pk_bf(cv[2 * e] - asf(h0), cv[2 * e + 1] - asf(h1));
          }
          *(LAS v4u*)(L + fo + a * 8192) = fh;
          *(LAS v4u*)(L + fo + a * 8192 + 256) = fl;
          SCHED();
        }
        if (a == C1AT) { issue_x(1); issue_w2(1); }
        if (a == C2AT) { issue_x(2); issue_w2(2); }
        STAMPX(3, 4 + a);
      }
    }
#define W1LATE 0
#define SMALLAT 1
    unsigned epoch_v;
    auto small_loads = [&]() __attribute__((always_inline)) {
    dma4(make_rsrc(P.bsp, 4096u), lane * 4u, (unsigned)p * 256u, lbase + L_BSP);
    {
      const unsigned wb_ = w == 0 ? (STWARM ? OFF_ST + (unsigned)(p * 14 + j) * 2048u : 0u) : w == 1 ? OFF_FA + (unsigned)j * 512u + (lane & 4) * (OFF_FY - OFF_FA) / 4 : w == 2 ? OFF_FH + (unsigned)p * 512u : w == 3 ? OFF_FW : OFF_FZ + (unsigned)(w - 4) * 1024u;
      const unsigned wm_ = w == 0 ? (STWARM ? 15u : 0u) : w < 3 ? 3u : 7u;
      const unsigned eo = lane == 63 ? 0u : wb_ + (lane & wm_) * 128u;
      asm volatile("buffer_load_dword %0, %1, %2, 0 offen sc1" : "=v"(epoch_v) : "v"(eo), "s"(wsr) : "memory");
    }
    };
    if (!SMALLAT) small_loads();
#define NSMALL 2
    STAMP(1);

    unsigned aoff[4], boff[4];
#pragma unroll
    for (int r = 0; r < 4; ++r) {
      aoff[r] = (32 * wm + r32) * 128 + (((2 * (r ^ wm) + h2) ^ ((r32 >> 1) & 7)) << 4);
      boff[r] = L_W2 + (16 * (r ^ wm) + 8 * h2 + trq) * 128 + ((wn ^ (trq >> 1)) << 6) + 32 * trb + 8 * trp;
      PIN(aoff[r]);
      PIN(boff[r]);
    }
    unsigned soff[2];
    soff[0] = (16 * w + (lane & 15)) * 128 + (((lane >> 4) ^ ((lane & 15) >> 1)) << 4);
    soff[1] = soff[0] ^ 64u;
    PIN(soff[0]); PIN(soff[1]);
    v4u xa[7][4];
    v4u wb[2][4], xs[2][2];
    f32x16 a2 = splat16(0.f);
    f32x4 sx1 = f32x4{0.f, 0.f, 0.f, 0.f}, sx2 = sx1, scs = sx1;
    v4u ones = v4u{0x3f803f80u, 0x3f803f80u, 0x3f803f80u, 0x3f803f80u};
    const unsigned selv = ((lane & 15) == ((lane >> 4) & 1)) ? 0x3f803f80u : 0u;
    v4u sel = v4u{selv, selv, selv, selv};
    PIN(ones); PIN(sel);
    const unsigned rowx = L_XS + (32 * wm + r32) * 8;
    const unsigned colw = (32 * wn + (lane & 15)) * 16 + wm * 4, colr = (32 * wn + r32) * 16 + h2 * 8;
#pragma unroll
    for (int i = 0; i < 7; ++i) {
      const int c = i - 1;
      if (!SMALLAT) { if (i < 3) wait_vm<6 + NSMALL>(); else if (i < 5) wait_vm<6>(); else if (i == 5) wait_vm<5>(); else wait_vm<4>(); }
      else if (!W1LATE) { if (i < 4) wait_vm<6>(); else if (i == 4) wait_vm<8>(); else if (i == 5) wait_vm<7>(); else wait_vm<6>(); }
      else { if (i < 4) wait_vm<6>(); else if (i == 4) wait_vm<8>(); else if (i == 5) wait_vm<5>(); else wait_vm<2>(); }
      STAMPX(2, 1 + 2 * i);
      bar();
      STAMPX(2, 2 + 2 * i);
      if (i == 0) { STAMP(2); STAMPX(3, 9); }
      if (i == 3 && !SMALLAT) GET_TAG(epoch_v);
#pragma unroll
      for (int r = 0; r < 4; ++r) {
        SCHED();
        if (i >= 1) a2 = mfma32(wb[c & 1][r], xa[c][r], a2);
        SCHED();
        if (r < 2) xs[i & 1][r] = *(LAS v4u*)(L + soff[r] + (i & 3) * 16384);
        wb[i & 1][r] = tr_frag(L, boff[r] + (i & 3) * 8192, boff[r] + (i & 3) * 8192 + 512);
        SCHED();
        if (i >= 1) {
          if (r & 1) sx2 = mfma16(xs[c & 1][r >> 1], xs[c & 1][r >> 1], sx2); else sx1 = mfma16(ones, xs[c & 1][r >> 1], sx1);
          if (r == 3) scs = mfma16(sel, wb[c & 1][0], scs);
        }
        SCHED();
        xa[i][r] = *(LAS v4u*)(L + aoff[r] + (i & 3) * 16384);
        SCHED();
        if (i <= 3) { if (r == 0) issue_x(i + 3); if (r == 2) issue_w2(i + 3); }
        if (!W1LATE && i >= 4 && (r & 1)) issue_w1(2 * (i - 4) + (r >> 1));
      }
      SCHED();
      if (SMALLAT && i == 3) small_loads();
    }
    sx1 = mfma16(ones, xs[0][0], sx1);
    sx2 = mfma16(xs[0][0], xs[0][0], sx2);
    scs = mfma16(sel, wb[0][0], scs);
    sx1 = mfma16(ones, xs[0][1], sx1);
    sx2 = mfma16(xs[0][1], xs[0][1], sx2);
    SCHED();
    a2 = mfma32(wb[0][0], xa[6][0], a2);
    a2 = mfma32(wb[0][1], xa[6][1], a2);
    SCHED();
    {
      const float d01 = (lane & 1) ? sx2[1] : sx2[0], d23 = (lane & 1) ? sx2[3] : sx2[2];
      const float dg = (lane & 2) ? d23 : d01;
      const float nm = sx1[0] * (-1.0f / 448.0f);
      const float ve = __builtin_fmaxf(dg * (1.0f / 448.0f) - nm * nm, 0.f) + 1e-5f;
      if (((lane & 15) >> 2) == (lane >> 4)) *(LAS f32x2*)(L + L_XS + (16 * w + (lane & 15)) * 8) = f32x2{nm, ve};
      if (lane < 16) {
        *(LAS float*)(L + L_CS2 + colw) = scs[0];
        *(LAS float*)(L + L_CS2 + colw + 256) = scs[1];
      }
    }
    SCHED();
    a2 = mfma32(wb[0][2], xa[6][2], a2);
    a2 = mfma32(wb[0][3], xa[6][3], a2);
    SCHED();
    STAMP(3);
    STAMPX(2, 15);
    bar();
    float nmu, rho;
    unsigned fv[4];
#pragma unroll
    for (int r = 0; r < 4; ++r) fv[r] = L_F + wn * 28672 + lane * 16 + (r ^ wm) * 1024;
    {
      const f32x2 t0 = *(LAS f32x2*)(L + rowx);
      const f32x2 c2 = *(LAS f32x2*)(L + L_CS2 + colr);
      if (!W1LATE) issue_w1(6);
#pragma unroll
      for (int r = 0; r < 4; ++r) wb[0][r] = *(LAS v4u*)(L + fv[r]);
      nmu = t0[0];
      a2 = __builtin_amdgcn_mfma_f32_32x32x2f32(c2[0] + c2[1], nmu, a2, 0, 0, 0);
      const float ve = t0[1];
      rho = __builtin_amdgcn_rsqf(ve);
      gsg = ve * rho;
      gk1 = -2.302208198f * rho;
      gk2 = -0.1029432397f * rho * rho * rho;
      gelu16(a2, gk1, gk2, gsg, [&](auto hc) __attribute__((always_inline)) { if (W1LATE == 2) issue_w1(5 + decltype(hc)::value); });
      float s1a = a2[0], s1b = a2[1], s2a = a2[0] * a2[0], s2b = a2[1] * a2[1];
#pragma unroll
      for (int e = 2; e < 16; e += 2) {
        s1a += a2[e]; s1b += a2[e + 1];
        s2a = __builtin_fmaf(a2[e], a2[e], s2a); s2b = __builtin_fmaf(a2[e + 1], a2[e + 1], s2b);
      }
      const auto sw = __builtin_amdgcn_permlane32_swap(asu(s1a + s1b), asu(s2a + s2b), false, false);
      const unsigned sw0 = sw[0], sw1 = sw[1];
      const float part = asf(sw0) + asf(sw1);
      if (wn) *(LAS float*)(L + L_PS + wm * 256 + lane * 4) = part;
      if (SMALLAT) { wait_vm<(W1LATE == 1 ? 0 : W1LATE == 2 ? 2 : 7)>(); GET_TAG(epoch_v); }
      bar();
      if (!wn) {
        const float tot = part + *(LAS float*)(L + L_PS + wm * 256 + lane * 4);
        st8_sc1<0>(v2u{asu(tot), tag}, wsr, r32 * 16u + h2 * 8u, OFF_ST + (unsigned)((p * 14 + j) * 128 + 32 * wm) * 16u);
      }
    }
    STAMP(4);
    const unsigned gso = OFF_ST + (unsigned)(p * 14) * 2048u;
    const unsigned gvo = (lane >> 4) * 2048u + (16 * w + (lane & 15)) * 16u, gv3 = gvo & ~4096u;
    v4u gq[4];
    auto issue_gather = [&]() __attribute__((always_inline)) {
#pragma unroll
      for (int a = 0; a < 4; ++a) ld16_sc1<0>(gq[a], wsr, a == 3 ? gv3 : gvo, gso + a * 8192);
    };
    unsigned yq[4];
    {
      f32x16 ay = splat16(0.f);
#pragma unroll
      for (int i = 1; i <= 7; ++i) {
#pragma unroll
        for (int r = 0; r < 4; ++r) {
          SCHED();
          ay = mfma32(wb[(i - 1) & 1][r], xa[i - 1][r], ay);
          SCHED();
          if (i < 7) wb[i & 1][r] = *(LAS v4u*)(L + fv[r] + i * 4096);
          if (W1LATE && i <= 4 && (r & 1) && 2 * (i - 1) + (r >> 1) < (W1LATE == 2 ? 5 : 7)) issue_w1(2 * (i - 1) + (r >> 1));
          if (i == GATH) ld16_sc1<0>(gq[r], wsr, r == 3 ? gv3 : gvo, gso + r * 8192);
        }
        SCHED();
      }
      const float dc = (j == 0 && wn == 0 && h2 == 0) ? 448.0f * nmu : 0.0f;
      yq[0] = pk_h((ay[0] + ay[8] + dc) * rho, (ay[1] + ay[9]) * rho);
#pragma unroll
      for (int q = 1; q < 4; ++q) yq[q] = pk_h((ay[2 * q] + ay[2 * q + 8]) * rho, (ay[2 * q + 1] + ay[2 * q + 9]) * rho);
      const unsigned yso = OFF_YT + (unsigned)(16 * j + 8 * wn) * 8192u + (unsigned)(128 * p + 32 * wm) * 4u;
      const unsigned yvo = h2 * 16384u + r32 * 4u;
      st4_sc1<0>(yq[0], wsr, yvo, yso);
      st4_sc1<0>(yq[1], wsr, yvo, yso + 8192);
      st4_sc1<0>(yq[2], wsr, yvo, yso + 32768);
      st4_sc1<0>(yq[3], wsr, yvo, yso + 40960);
    }
    STAMP(5);
    {
      wait_vm<4>();
#pragma unroll 1
      for (unsigned spins = 0;; ++spins) {
        asm volatile("" : "+v"(gq[0]), "+v"(gq[1]), "+v"(gq[2]), "+v"(gq[3])::"memory");
        unsigned bad = 0;
#pragma unroll
        for (int a = 0; a < 4; ++a) bad |= (gq[a][1] ^ tag) | (gq[a][3] ^ tag);
        if (wave_all(bad == 0)) break;
        if (spins > SPIN_CAP) { st4_sc1<0>(1u, wsr, OFF_DBG, 0); break; }
        issue_gather();
        wait_vm<0>();
      }
      STAMPX(1, 14);
      const float s1 = (asf(gq[0][0]) + asf(gq[1][0])) + (asf(gq[2][0]) + (lane < 32 ? asf(gq[3][0]) : 0.f));
      const float s2 = (asf(gq[0][2]) + asf(gq[1][2])) + (asf(gq[2][2]) + (lane < 32 ? asf(gq[3][2]) : 0.f));
      const auto sw = __builtin_amdgcn_permlane32_swap(asu(s1), asu(s2), false, false);
      const unsigned sw0 = sw[0], sw1 = sw[1];
      const float tot = row_pair_sum(asf(sw0) + asf(sw1));
      *(LAS float*)(L + L_ZS + (16 * w + (lane & 15)) * 8 + h2 * 4) = tot;
    }
    bar();
    {
      const f32x2 t0 = *(LAS f32x2*)(L + rowx + (L_ZS - L_XS));
      const float mean = t0[0] * (1.0f / 896.0f);
      const float rs = __builtin_amdgcn_rsqf(__builtin_fmaxf(t0[1] * (1.0f / 896.0f) - mean * mean, 0.f) + 1e-5f);
      const float mo = -mean * rs;
      const unsigned tw = L_Z1 + w * 2048 + r32 * 64;
#pragma unroll
      for (int g = 0; g < 4; ++g) {
        v2u o;
        o[0] = pk_bf(__builtin_fmaf(a2[4 * g], rs, mo), __builtin_fmaf(a2[4 * g + 1], rs, mo));
        o[1] = pk_bf(__builtin_fmaf(a2[4 * g + 2], rs, mo), __builtin_fmaf(a2[4 * g + 3], rs, mo));
        *(LAS v2u*)(L + tw + (((2 * g + h2) ^ ((r32 >> 1) & 7)) << 3)) = o;
      }
      FENCE();
      const unsigned tr0 = L_Z1 + w * 2048 + (8 * (lane >> 4) + trq) * 64 + ((trp ^ (4 * trb + (trq >> 1))) << 3);
      const unsigned tr1 = (tr0 ^ 16u) + 256;
      const v4u z0 = tr_frag(L, tr0, tr1), z1f = tr_frag(L, tr0 ^ 32u, tr1 ^ 32u);
      const unsigned zso = OFF_Z2N + (unsigned)((j * 64 + 4 * p + wm) * 4 + 2 * wn) * 1024u;
      st16_sc1<0>(z0, wsr, lane * 16u, zso);
      st16_sc1<1024>(z1f, wsr, lane * 16u, zso);
    }
    STAMP(6);
    wait_vm<2>();
    if (grp_arrive_last(L, L_CTR + 44, 8u)) {
      if (lane == 0) st4_sc1<0>(tag, wsr, OFF_FY + (unsigned)(j * 16 + p) * 32u, 0);
    }
    auto z1_loop = [&](auto&& hook) __attribute__((always_inline)) {
      scs = f32x4{0.f, 0.f, 0.f, 0.f};
#pragma unroll
      for (int r = 0; r < 4; ++r) { boff[r] -= L_W2; PIN(boff[r]); }
      static_for<0, 32>([&](auto tc) __attribute__((always_inline)) {
        constexpr int t = decltype(tc)::value, i = t >> 2, r = t & 3, c = i - 1;
        SCHED();
        if constexpr (i >= 1) {
          a1 = mfma32(wb[c & 1][r], xa[c][r], a1);
          if constexpr (r == 0) scs = mfma16(sel, wb[c & 1][0], scs);
        }
        SCHED();
        if constexpr (i < 7) wb[i & 1][r] = tr_frag(L, boff[r] + w1_base(i), boff[r] + w1_base(i) + 512);
        SCHED();
        hook(tc);
      });
      if (lane < 16) {
        *(LAS float*)(L + L_CS1 + colw) = scs[0];
        *(LAS float*)(L + L_CS1 + colw + 256) = scs[1];
      }
      bar();
      STAMP(8);
      const f32x2 c2 = *(LAS f32x2*)(L + L_CS1 + colr);
      a1 = __builtin_amdgcn_mfma_f32_32x32x2f32(c2[0] + c2[1], nmu, a1, 0, 0, 0);
    };
    auto publish_z2n = [&]() __attribute__((always_inline)) {
      if (grp_arrive_last(L, L_CTR + 16, 8u)) {
        if (lane == 0) st4_sc1<0>(tag, wsr, OFF_FA + (unsigned)(j * 16 + p) * 32u, 0);
      }
      STAMP(7);
    };
#define YASYNC 1
    if (w >= 4) {
      unsigned yff = 0;
      z1_loop([&](auto tc) __attribute__((always_inline)) {
        constexpr int t = decltype(tc)::value;
        if constexpr (t == FLAGT) { wait_vm<0>(); publish_z2n(); }
        if constexpr (t == YLT - 8) ld4_sc1<0>(yff, wsr, fft_flags, 0);
        if constexpr (t == YLT) { wait_vm<0>(); unsigned& y_ = yff; PIN(y_); if (!wave_all(yff == tag)) poll_flags<1>(wsr, fft_flags, true, tag, 3u); load_fft_in(); }
      });
      FFT_IN_LANDED();
    }
    int w_opaque = w;
    asm volatile("" : "+s"(w_opaque));
    if (w_opaque < 4) {
#define ROTMASK 0
      const unsigned rot = (j + 2 * p) & ROTMASK;
      const v4u spr = make_rsrc(P.wsp, 2048u * 4096u);
      const unsigned spv0 = (unsigned)(128 * p + (lane >> 3)) * 4096u + (((lane & 7) ^ (lane >> 4)) << 4);
      const unsigned spv1 = (spv0 ^ 64u) + 8u * 4096u - 1024u, spv2 = spv0 + 16u * 4096u - 2048u, spv3 = (spv0 ^ 64u) + 24u * 4096u - 3072u;
      auto sp_soff = [&](int u) __attribute__((always_inline)) { return (unsigned)w * 1024u + (((u >> 2) ^ rot) * 128) + (unsigned)((u + 1 + w) & 3) * (32 * 4096); };
      auto issue_sp = [&](int u) __attribute__((always_inline)) { dma16x4(spr, spv0, spv1, spv2, spv3, sp_soff(u), lbase + ring_base(u % 7) + w * 4096); };
      z1_loop([&](auto tc) __attribute__((always_inline)) {
        constexpr int t = decltype(tc)::value, q = t - 4;
        if constexpr (q >= 0 && q < 12) {
          if constexpr ((q & 3) == 0) set_m0(lbase + ring_base(q >> 2) + w * 4096);
          dma_piece<(q & 3) * 1024>(spr, (q & 3) == 0 ? spv0 : (q & 3) == 1 ? spv1 : (q & 3) == 2 ? spv2 : spv3, sp_soff(q >> 2));
        }
        if constexpr (t == FLAGT) { wait_vm<12>(); publish_z2n(); }
      });
      if (NPRE > 3) issue_sp(3);
      if (NPRE > 6) issue_sp(4);
      finish_z1([&](auto hc) __attribute__((always_inline)) { constexpr int u = (NPRE > 6 ? 5 : 4) + decltype(hc)::value; if constexpr (u < NPRE) issue_sp(u); });
      __builtin_amdgcn_s_setprio(GPRIO_);
      unsigned rdl[2], rdh[2];
#pragma unroll
      for (int ks = 0; ks < 2; ++ks) {
        rdl[ks] = w * 4096 + (lane & 15) * 128 + (((4 * ks + (lane >> 4)) ^ ((lane & 15) >> 1)) << 4);
        rdh[ks] = rdl[ks] + RING_HI;
        PIN(rdl[ks]);
        PIN(rdh[ks]);
      }
      poll_flags<ASL>(wsr, OFF_FA + (unsigned)(j * 4 + w) * 128u + (lane & 3) * 32u, true, tag, 2u);
      STAMP(10);
      const unsigned zso = OFF_Z2N + (unsigned)(j * 128 + 32 * w) * 2048u;
      const unsigned zv = lane * 16u;
      v4u bz[2][8];
      auto issue_b = [&](int c) __attribute__((always_inline)) {
        v4u(&d)[8] = bz[c & 1];
        const unsigned so = zso + (c ^ rot) * 8192;
        ld16_sc1<0>(d[0], wsr, zv, so); ld16_sc1<1024>(d[1], wsr, zv, so); ld16_sc1<2048>(d[2], wsr, zv, so); ld16_sc1<3072>(d[3], wsr, zv, so);
        ld16_sc1<0>(d[4], wsr, zv, so + 4096); ld16_sc1<1024>(d[5], wsr, zv, so + 4096); ld16_sc1<2048>(d[6], wsr, zv, so + 4096); ld16_sc1<3072>(d[7], wsr, zv, so + 4096);
      };
      issue_b(0);
      issue_b(1);
      static_for<NPRE, 7>([&](auto uc) __attribute__((always_inline)) { issue_sp(decltype(uc)::value); });
      STAMP(9);
      f32x4 acc[4][4][2];
#pragma unroll
      for (int tt = 0; tt < 2; ++tt) {
        const float bias = bf_lo(*(LAS unsigned short*)(L + L_BSP + (32 * w + 16 * tt + (lane & 15)) * 2));
#pragma unroll
        for (int ft = 0; ft < 4; ++ft) {
          acc[3][ft][tt] = f32x4{bias, bias, bias, bias};
#pragma unroll
          for (int b = 0; b < 3; ++b) acc[b][ft][tt] = f32x4{0.f, 0.f, 0.f, 0.f};
        }
      }
      v4u af[2][4];
      unsigned dmp;
      static_for<0, 33>([&](auto sc) __attribute__((always_inline)) {
        constexpr int s = decltype(sc)::value;
        constexpr int u = s - 1, c = u >> 2, rg = u & 3;
        v4u(&d)[8] = bz[c & 1];
        v4u(&dn)[8] = bz[(c + 1) & 1];
        if constexpr (s >= 1) asm volatile("" : "+v"(af[u & 1][0]), "+v"(af[u & 1][1]), "+v"(af[u & 1][2]), "+v"(af[u & 1][3])::"memory");
        if constexpr (s < 32) {
          constexpr int need = p2_pos(s) - p2_dma_done(s);
          asm volatile("s_waitcnt vmcnt(%0)" ::"i"(need > 63 ? 63 : need) : "memory");
          constexpr unsigned rb = ring_base(s % 7);
#pragma unroll
          for (int q = 0; q < 4; ++q) af[s & 1][q] = *(LAS v4u*)(L + (rb >= RING_HI ? rdh[q >> 1] + (rb - RING_HI) : rdl[q >> 1] + rb) + (q & 1) * 2048);
        }
        if constexpr (s == 28) dmp = w * 4096 + fresh_lane() * 16;
        if constexpr (s == A3S || s == 29) grp_arrive(L, L_CTR + 24 + 4 * w);
        SCHED();
        if constexpr (s >= 1) {
          if constexpr (rg == 0) {
            constexpr int need = p2_pos(s) - p2_b_done(c);
            asm volatile("s_waitcnt vmcnt(%8)"
                         : "+v"(d[0]), "+v"(d[1]), "+v"(d[2]), "+v"(d[3]), "+v"(d[4]), "+v"(d[5]), "+v"(d[6]), "+v"(d[7])
                         : "i"(need > 63 ? 63 : need)
                         : "memory");
          }
          const unsigned so = sp_soff(u + 7);
          const unsigned bo = zso + ((c + 1) ^ rot) * 8192 + rg * 4096;
          if constexpr (p2_dma_in(s)) set_m0(lbase + ring_base(u % 7) + w * 4096);
          static_for<0, 4>([&](auto kc) __attribute__((always_inline)) {
            constexpr int ks = decltype(kc)::value;
            acc[rg][(2 * ks) & 3][0] = mfma16(d[2 * ks], af[u & 1][ks & 2], acc[rg][(2 * ks) & 3][0]);
            acc[rg][(2 * ks) & 3][1] = mfma16(d[2 * ks], af[u & 1][(ks & 2) + 1], acc[rg][(2 * ks) & 3][1]);
            SCHED();
            if constexpr (p2_dma_in(s)) dma_piece<ks * 1024>(spr, ks == 0 ? spv0 : ks == 1 ? spv1 : ks == 2 ? spv2 : spv3, so);
            SCHED();
            acc[rg][(2 * ks + 1) & 3][0] = mfma16(d[2 * ks + 1], af[u & 1][ks & 2], acc[rg][(2 * ks + 1) & 3][0]);
            acc[rg][(2 * ks + 1) & 3][1] = mfma16(d[2 * ks + 1], af[u & 1][(ks & 2) + 1], acc[rg][(2 * ks + 1) & 3][1]);
            SCHED();
            if constexpr (p2_b_in(s) == 4) ld16_sc1<ks * 1024>(dn[4 * rg + ks], wsr, zv, bo);
            if constexpr (p2_b_in(s) == 8) { ld16_sc1<(ks & 1) * 2048>(dn[2 * ks], wsr, zv, bo + (ks >> 1) * 4096); ld16_sc1<(ks & 1) * 2048 + 1024>(dn[2 * ks + 1], wsr, zv, bo + (ks >> 1) * 4096); }
            if constexpr (s >= 30) {
              const f32x4 &t0 = acc[rg - 1][ks & 2][ks & 1], &t1 = acc[rg - 1][(ks & 2) + 1][ks & 1];
              *(LAS v4u*)(L + dmp + ring_base(rg) + ks * 1024) = v4u{pk_bf(t0[0], t0[1]), pk_bf(t0[2], t0[3]), pk_bf(t1[0], t1[1]), pk_bf(t1[2], t1[3])};
            }
            SCHED();
          });
        }
      });
      grp_arrive(L, L_CTR + 0);
      STAMP(11);
      STAMPX(4, 1);
      RELANE();
      v4u es[2];
      {
        const unsigned one = (lane & 1) ? 0x3f800000u : 0x00003f80u;
        const unsigned on = trq == (lane >> 4) ? one : 0u;
        const unsigned e0 = (lane & 2) ? 0u : on, e1 = (lane & 2) ? on : 0u;
        es[0] = v4u{e0, e1, 0u, 0u};
        es[1] = v4u{0u, 0u, e0, e1};
      }
      unsigned pa[3], za = L_Z1 + w * 2048 + (trb * 32 + (lane & 15)) * 16 + h2 * 8, hv = h2 * 1024 + (trb * 32 + (lane & 15)) * 16, fo_h;
#pragma unroll
      for (int rel = 1; rel < 4; ++rel) { pa[rel - 1] = ring_base(rel) + ((w - rel) & 3) * 4096 + lane * 16; PIN(pa[rel - 1]); }
      {
        const int k0 = (4 * j) / 14, k1 = (4 * j + 3) / 14;
        const unsigned f0 = OFF_FH + (unsigned)(p * 4 + k0) * 128u + (unsigned)(j - (7 * k0) / 2) * 32u, f1 = OFF_FH + (unsigned)(p * 4 + k1) * 128u + (unsigned)(j - (7 * k1) / 2) * 32u;
        fo_h = lane ? f1 : f0;
      }
      PIN(es[0]); PIN(es[1]); PIN(za); PIN(hv); PIN(fo_h);
      const unsigned hso = OFF_H + (unsigned)((p * 4 + w) * 56 + 4 * j) * 1024u;
      grp_wait(L, L_CTR + 0, 8u);
      STAMP(19);
      {
        v4u pt[3][4];
        v2u z1q[8];
#pragma unroll
        for (int q = 0; q < 4; ++q) {
#pragma unroll
          for (int rel = 1; rel < 4; ++rel) pt[rel - 1][q] = *(LAS v4u*)(L + pa[rel - 1] + q * 1024);
          if (q == 1) {
#pragma unroll
            for (int a = 0; a < 8; ++a) z1q[a] = *(LAS v2u*)(L + za + (a >> 2) * 8192 + ((a >> 1) & 1) * 256 + (a & 1) * 1024);
          }
        }
        grp_arrive(L, L_CTR + 20);
        SCHED();
        float zf[32];
#pragma unroll
        for (int q = 0; q < 4; ++q)
#pragma unroll
          for (int rel = 0; rel < 3; ++rel) {
#pragma unroll
            for (int b = 0; b < 2; ++b) acc[3][(q & 2) + b][q & 1] = mfma16(es[b], pt[rel][q], acc[3][(q & 2) + b][q & 1]);
            SCHED();
            if (3 * q + rel >= 4) {
              const int a = 3 * q + rel - 4;
#pragma unroll
              for (int e = 0; e < 2; ++e) {
                zf[4 * a + 2 * e] = bf_lo(z1q[a][e]); zf[4 * a + 2 * e + 1] = bf_hi(z1q[a][e]);
                PIN(zf[4 * a + 2 * e]); PIN(zf[4 * a + 2 * e + 1]);
              }
            }
            SCHED();
          }
        static_for<0, 4>([&](auto qc) __attribute__((always_inline)) {
          constexpr int q = decltype(qc)::value;
          v4u hq;
#pragma unroll
          for (int e = 0; e < 4; ++e) {
            const f32x4& t = acc[3][(q & 2) + (e >> 1)][q & 1];
            hq[e] = pk_bf(zf[8 * q + 2 * e] * t[2 * (e & 1)], zf[8 * q + 2 * e + 1] * t[2 * (e & 1) + 1]);
          }
          st16_sc1<(q >> 1) * 2048 + (q & 1) * 256>(hq, wsr, hv, hso);
          SCHED();
        });
      }
      STAMP(12);
      STAMPX(4, 2);
      wait_vm<0>();
      if (grp_arrive_last(L, L_CTR + 4, 4u)) {
        if (lane < 2) st4_sc1<0>(tag, wsr, fo_h, 0);
      }
      __builtin_amdgcn_s_setprio(0);
      STAMP(13);
      asm volatile("" : "=v"(a1), "=v"(gk1), "=v"(gk2), "=v"(gsg));
      asm volatile("" : "=v"(yin[0]), "=v"(yin[1]), "=v"(yin[2]), "=v"(yin[3]), "=v"(yin[4]), "=v"(yin[5]), "=v"(yin[6]), "=v"(yin[7]));
    }
  } else {
    if (tid < 16) *(LAS unsigned*)(L + L_CTR + 4 * tid) = 0u;
    const bool wworker = (rr ^ xcd) & 1;
    {
      unsigned epoch_v;
      asm volatile("buffer_load_dword %0, off, %1, 0 sc1" : "=v"(epoch_v) : "s"(wsr) : "memory");
      if (wworker) {
        const int rh = sidx >> 4, lb = (sidx >> 1) & 7;
        const unsigned v0 = lane * 16u;
        const v4u rs = make_rsrc(w < 4 ? P.wr : P.wi, 2048u * 450u);
        const unsigned so = (unsigned)(32 * lb + 256 * (4 * rh + (w & 3))) * 450u;
#pragma unroll
        for (int a = 0; a < 3; ++a) dma16x4(rs, v0, v0, v0, v0, so + a * 4096, lbase + w * 16384 + a * 4096);
        dma16x2(rs, v0, v0, so + 12288, lbase + w * 16384 + 12288);
        dma16(rs, v0, so + 14336, lbase + w * 16384 + 14336);
        wait_vm<0>();
        bar();
        const unsigned q = (tid >> 3) & 3, li = tid & 7;
        const unsigned a0 = (4 * li + q) * 450, a1 = a0 + 65536;
#pragma unroll 1
        for (int hf = 0; hf < 3; ++hf) {
          unsigned short e[5][8];
          unsigned kk[5];
#pragma unroll
          for (int it = 0; it < 5; ++it) {
            kk[it] = (tid >> 5) + 16 * (5 * hf + it);
            kk[it] = kk[it] < 225 ? kk[it] : 224;
#pragma unroll
            for (int r = 0; r < 8; ++r) e[it][r] = *(LAS unsigned short*)(L + (r < 4 ? a0 : a1) + kk[it] * 2 + (r & 3) * 16384);
          }
          SCHED();
#pragma unroll
          for (int it = 0; it < 5; ++it) {
            v4u o;
#pragma unroll
            for (int r = 0; r < 4; ++r) o[r] = (unsigned)e[it][r] | ((unsigned)e[it][4 + r] << 16);
            st16_sc1<0>(o, wsr, (((kk[it] * 4 + q) * 2 + rh) * 64 + 8 * lb + li) * 16u, OFF_WT);
          }
        }
      }
      wait_vm<0>();
      GET_TAG(epoch_v);
      bar();
      if (tid == 0) st4_sc1<0>(tag, wsr, OFF_FW + (unsigned)sidx * 32u, 0);
      bar();
      STAMP(8);
    }
    const bool nyq = sidx == 1;
    if (nyq && w < 4) { STAMP_OUT(); return; }
    if (!nyq) {
      unsigned dmy = 0;
      const v4u spr = make_rsrc(P.wsp, 2048u * 4096u);
      const unsigned vo = lane * 4096u, so = (unsigned)(128 * p + 64 * (w >> 2)) * 4096u + (unsigned)(w & 3) * 1024u;
#pragma unroll
      for (int i = 0; i < 8; ++i) {
        asm volatile("buffer_load_dword %0, %1, %2, %3 offen" : "+v"(dmy) : "v"(vo), "s"(spr), "s"(so + (((wworker ? LATE0 : 0) + i) & 7) * 128u) : "memory");
        if (i == 7 - LATE0 && wworker) {
          const v4u wor = make_rsrc(P.wout, 896u * 896u);
          asm volatile("buffer_load_dword %0, %1, %2, %3 offen" : "+v"(dmy) : "v"(lane * 128u), "s"(wor), "s"((unsigned)((sidx >> 1) * 8 + w) * 8192u) : "memory");
        }
      }
      STAMP(9);
#define WOL2 1
      {
        asm volatile("s_waitcnt vmcnt(0)" : "+v"(dmy)::"memory");
        const v4u wor = make_rsrc(P.wout, 896u * 896u);
#pragma unroll
        for (int i = 0; i < 3; ++i)
          asm volatile("buffer_load_dword %0, %1, %2, %3 offen" : "+v"(dmy) : "v"(lane * 128u), "s"(wor), "s"((unsigned)(((rr & 3) * 8 + w) * 3 + i) * 8192u) : "memory");
      }
      asm volatile("s_waitcnt vmcnt(0)" : "+v"(dmy)::"memory");
      STAMP(10);
      STAMP_OUT();
      return;
    }
    poll_flags<8>(wsr, fft_flags, true, tag, 3u);
    load_fft_in();
    FFT_IN_LANDED();
  }

  if (w >= 4) {
#define FPRIO 2
#define FPRIO_C 0
#define FPRIO_A 0
#define FPRIO_G 0
    __builtin_amdgcn_s_setprio(FPRIO);
    const int fw = w - 4;
    const unsigned tp = tid - 256;
    const int mode = is_main ? (slot == 0 ? 1 : 0) : 2;
    const unsigned myreg = L_FFT + 4096u * fw;
    unsigned fwf;
    ld4_sc1<0>(fwf, wsr, OFF_FW + (lane & 31) * 32u, 0);
    cf t1[8], tb[7], tc[7];
    FftAddr FA;
    {
      const float a1 = (float)(fw * lane) * (1.0f / 2048.0f), d1 = (float)fw * (1.0f / 32.0f);
      const float ab = (float)(lane & 7) * (1.0f / 64.0f), ac = (float)lane * (1.0f / 512.0f);
#pragma unroll
      for (int r = 0; r < 8; ++r) {
        const float a = __builtin_fmaf((float)r, d1, a1);
        t1[r] = cf{__builtin_amdgcn_cosf(a) * 0.022097086912079612f, __builtin_amdgcn_sinf(a) * 0.022097086912079612f};
        PIN(t1[r].x); PIN(t1[r].y);
        FA.wa[r] = myreg + 8 * fsw(8 * lane + r);
        FA.wb[r] = myreg + 8 * fsw((lane >> 3) * 64 + (lane & 7) + 8 * r);
        PIN(FA.wa[r]); PIN(FA.wb[r]);
      }
#pragma unroll
      for (int r = 1; r < 8; ++r) {
        const float a = r * ab, b = r * ac;
        tb[r - 1] = cf{__builtin_amdgcn_cosf(a), __builtin_amdgcn_sinf(a)};
        tc[r - 1] = cf{__builtin_amdgcn_cosf(b), __builtin_amdgcn_sinf(b)};
        PIN(tb[r - 1].x); PIN(tb[r - 1].y); PIN(tc[r - 1].x); PIN(tc[r - 1].y);
      }
      FA.rd[0] = myreg + 8 * fsw(lane);
      FA.rd[1] = myreg + 8 * fsw(lane + 64) - 512;
      PIN(FA.rd[0]); PIN(FA.rd[1]);
    }
    unsigned e1 = L_FFT + 8 * fsw(tp), e2a = L_FFT + 8 * fsw(2 * tp), e2b = e2a ^ 8u;
    PIN(e1); PIN(e2a); PIN(e2b);
    wait_vm<0>();
    PIN(fwf);
    if (!wave_all(fwf == tag)) poll_flags<2>(wsr, OFF_FW + (lane & 31) * 32u, true, tag, 3u);
    v4u wq[2];
    ld16_sc1<0>(wq[0], wsr, lane * 16u, OFF_WT + (unsigned)(kw * 4 + fw) * 2048u);
    ld16_sc1<1024>(wq[1], wsr, lane * 16u, OFF_WT + (unsigned)(kw * 4 + fw) * 2048u);
    STAMP(10);
    __builtin_amdgcn_s_setprio(FPRIO_C);
    if (mode != 0) {
#pragma unroll
      for (int m = 0; m < 8; ++m) yin[m] = mode == 2 ? yin[m] >> 16 : yin[m] & 0xffffu;
    }
    cf v[8];
#pragma unroll
    for (int m = 0; m < 8; ++m) {
      const h16x2 hv = __builtin_bit_cast(h16x2, yin[m]);
      v[m] = cf{(float)hv[0], (float)hv[1]};
    }
    dft4<-1>(v[0], v[2], v[4], v[6]);
    dft4<-1>(v[1], v[3], v[5], v[7]);
#pragma unroll
    for (int q = 0; q < 4; ++q) {
      lds_wc(L, e1 + 4096 * q, v[2 * q]);
      lds_wc(L, e1 + 4096 * q + 2048, v[2 * q + 1]);
    }
    grp_sync(L, L_CTR + 8, 4u);
#pragma unroll
    for (int r = 0; r < 8; ++r) v[r] = ctw<-1>(lds_rc(L, FA.rd[r & 1] + 512 * r), t1[r]);
    FENCE();
    fft512<-1>(v, tb, tc, L, FA);
    SCHED();
    wait_vm<0>();
    asm volatile("" : "+v"(wq[0]), "+v"(wq[1]));
#pragma unroll
    for (int r = 0; r < 8; ++r) {
      const unsigned u = wq[r >> 2][r & 3];
      v[r] = ctw<1>(v[r], cf{bf_lo(u), bf_hi(u)});
    }
    fft512<1>(v, tb, tc, L, FA);
#pragma unroll
    for (int r = 0; r < 8; ++r) lds_wc(L, FA.rd[r & 1] + 512 * r, ctw<1>(v[r], t1[r]));
    grp_sync(L, L_CTR + 8, 8u);
#pragma unroll
    for (int q = 0; q < 4; ++q) {
      v[2 * q] = lds_rc(L, e2a + 4096 * q);
      v[2 * q + 1] = lds_rc(L, e2b + 4096 * q);
    }
    dft4<1>(v[0], v[2], v[4], v[6]);
    dft4<1>(v[1], v[3], v[5], v[7]);
    __builtin_amdgcn_s_setprio(FPRIO);
    {
      const unsigned so = OFF_ZT + (unsigned)slot * 8192u;
      if (mode == 0) {
        st8_sc1<0>(v2u{pk_h(v[0].x, v[0].y), pk_h(v[1].x, v[1].y)}, wsr, tp * 8u, so);
        st8_sc1<2048>(v2u{pk_h(v[2].x, v[2].y), pk_h(v[3].x, v[3].y)}, wsr, tp * 8u, so);
        st8_sc1<0>(v2u{pk_h(v[4].x, v[4].y), pk_h(v[5].x, v[5].y)}, wsr, tp * 8u, so + 4096);
        st8_sc1<2048>(v2u{pk_h(v[6].x, v[6].y), pk_h(v[7].x, v[7].y)}, wsr, tp * 8u, so + 4096);
      } else {
        const unsigned vo = tp * 8u + (mode == 2 ? 2u : 0u);
        st2_sc1<0>(pk_h(v[0].x, 0.f), wsr, vo, so); st2_sc1<4>(pk_h(v[1].x, 0.f), wsr, vo, so);
        st2_sc1<2048>(pk_h(v[2].x, 0.f), wsr, vo, so); st2_sc1<2052>(pk_h(v[3].x, 0.f), wsr, vo, so);
        st2_sc1<0>(pk_h(v[4].x, 0.f), wsr, vo, so + 4096); st2_sc1<4>(pk_h(v[5].x, 0.f), wsr, vo, so + 4096);
        st2_sc1<2048>(pk_h(v[6].x, 0.f), wsr, vo, so + 4096); st2_sc1<2052>(pk_h(v[7].x, 0.f), wsr, vo, so + 4096);
      }
    }
    STAMP(11);
    const unsigned g4 = lane >> 4, n16 = lane & 15;
    v4u gf[4][4];
    auto make_g = [&](int i) __attribute__((always_inline)) {
      int u = 4 * i + fw;
      u = u < 14 ? u : 13;
      const int ks = u < 2 ? u + 12 : u - 2;
#pragma unroll
      for (int d4 = 0; d4 < 4; ++d4) {
        const unsigned dcol = 64 * dt + 16 * ((d4 + fw) & 3) + n16;
        const float df = (float)dcol, fa = rev448(df, (float)(16 * ks + 4 * g4)), fs = df * (1.0f / 448.0f);
#pragma unroll
        for (int e = 0; e < 4; ++e) {
          const float a = __builtin_fmaf((float)e, fs, fa);
          gf[i][d4][e] = pk_h(__builtin_amdgcn_cosf(a), __builtin_amdgcn_sinf(-a));
        }
        if (i == 0) {
          if (fw == 2 && g4 == 0) gf[0][d4][0] = (n16 & 1) ? 0xb8003800u : 0x38003800u;
        }
        PIN(gf[i][d4]);
      }
    };
    __builtin_amdgcn_s_setprio(FPRIO_G);
    if (is_main) {
      finish_z1([](auto) {});
      grp_arrive(L, L_CTR + 0);
    }
    __builtin_amdgcn_s_setprio(FPRIO);
    wait_vm<0>();
    if (grp_arrive_last(L, L_CTR + 12, 4u)) {
      if (lane == 0) st4_sc1<0>(tag, wsr, OFF_FZ + (unsigned)kw * 16u, 0);
    }
    STAMP(12);
    if (!is_main) {
      STAMP_OUT();
      return;
    }
    __builtin_amdgcn_s_setprio(FPRIO_G);
    make_g(0);
    make_g(1);
    make_g(2);
    make_g(3);
    __builtin_amdgcn_s_setprio(FPRIO);
    grp_sync(L, L_CTR + 8, 12u);
    STAMP(13);
    __builtin_amdgcn_s_setprio(0);
    grp_wait_sleep(L, L_CTR + 24 + 4 * fw, 1u);
    __builtin_amdgcn_s_setprio(FPRIO);
    {
      const unsigned ug = 4 * g4 + fw;
      const unsigned kg = ug < 2 ? ug + 12 : ug - 2;
      const unsigned fo = ug < 14 ? kg * 256u + n16 * 16u : 224u * 16u;
      poll_flags<6>(wsr, OFF_FZ + fo, ug <= 14, tag, 5u);
    }
    STAMP(19);
    STAMPX(4, 1);
    v4u zr[4][4];
    {
      const unsigned vo = g4 * 32768u + n16 * 16u;
#pragma unroll
      for (int i = 0; i < 4; ++i) {
        int u = 4 * i + fw;
        u = u < 14 ? u : 13;
        const int ks = u < 2 ? u + 12 : u - 2;
        const unsigned so = OFF_ZT + (unsigned)ks * (16u * 8192u) + (unsigned)(128 * p + 64 * th) * 4u;
#pragma unroll
        for (int c = 0; c < 4; ++c) ld16_sc1<0>(zr[i][c], wsr, (i == 3 && fw >= 2) ? WS_BYTES : vo, so + c * 8192);
      }
      const unsigned rv = (unsigned)(128 * p + 64 * th + 16 * fw + (lane >> 3)) * 896u + (unsigned)dt * 128u + (lane & 7) * 16u;
      dma16x2(xr, rv, rv + 8u * 896u - 1024u, 0, lbase + L_XR + fw * 2048);
    }
    const v4u wor = make_rsrc(P.wout, 896u * 896u);
    const unsigned ov0 = (lane >> 3) * 896u + (unsigned)dt * 128u + (((lane & 7) ^ (((lane >> 4) & 1) << 2)) << 4);
    const unsigned ov1 = ov0 + 8u * 896u - 1024u, ov2 = ov0 + 16u * 896u - 2048u, ov3 = ov0 + 24u * 896u - 3072u;
    auto issue_wo = [&](int i) __attribute__((always_inline)) { dma16x4(wor, ov0, ov1, ov2, ov3, (unsigned)((7 * fw + i) * 32) * 896u, lbase + ring_base(i) + fw * 4096); };
    __builtin_amdgcn_s_setprio(FPRIO_A);
    grp_wait(L, L_CTR + 24 + 4 * fw, 2u);
    STAMPX(4, 2);
    f32x4 sacc[4][4];
#pragma unroll
    for (int a = 0; a < 4; ++a)
#pragma unroll
      for (int b = 0; b < 4; ++b) sacc[a][b] = f32x4{0.f, 0.f, 0.f, 0.f};
    static_for<0, 4>([&](auto ic) __attribute__((always_inline)) {
      constexpr int i = decltype(ic)::value, sl = (4 + i) % 7;
      v4u(&z4)[4] = zr[i];
      asm volatile("s_waitcnt vmcnt(14)" : "+v"(z4[0]), "+v"(z4[1]), "+v"(z4[2]), "+v"(z4[3])::"memory");
      set_m0(lbase + ring_base(sl) + fw * 4096);
      static_for<0, 4>([&](auto tc) __attribute__((always_inline)) {
        constexpr int ti = decltype(tc)::value;
        const v4u zf = v4u{z4[0][ti], z4[1][ti], z4[2][ti], z4[3][ti]};
#pragma unroll
        for (int d4 = 0; d4 < 4; ++d4) sacc[ti][d4] = mfma_h(gf[i][d4], zf, sacc[ti][d4]);
        SCHED();
        dma_piece<ti * 1024>(wor, ti == 0 ? ov0 : ti == 1 ? ov1 : ti == 2 ? ov2 : ov3, (unsigned)((7 * fw + sl) * 32) * 896u);
        SCHED();
      });
    });
#pragma unroll
    for (int r = 0; r < 4; ++r) {
      const unsigned sb = L_FFT + 4096u * ((fw + r) & 3) + lane * 16;
      STAMPX(4, 3 + 2 * r);
      if (r > 0) {
        grp_wait(L, L_CTR + 8, 12u + 4u * r);
        f32x4 t[4];
#pragma unroll
        for (int ti = 0; ti < 4; ++ti) t[ti] = *(LAS f32x4*)(L + sb + ti * 1024);
        SCHED();
#pragma unroll
        for (int ti = 0; ti < 4; ++ti) sacc[ti][r] += t[ti];
      }
#pragma unroll
      for (int ti = 0; ti < 4; ++ti) *(LAS f32x4*)(L + sb + ti * 1024) = sacc[ti][r];
      if (r < 3) grp_arrive(L, L_CTR + 8);
      STAMPX(4, 4 + 2 * r);
      if (r == WOR) {
        grp_wait(L, L_CTR + 20, 4u);
        issue_wo(1); issue_wo(2); issue_wo(3);
      }
    }
    __builtin_amdgcn_s_setprio(0);
    STAMPX(1, 9);
  }

  RELANE();
  const int tp2 = w & 1, kq = w >> 1;
  unsigned ob[2][2];
#pragma unroll
  for (int d2 = 0; d2 < 2; ++d2) {
    ob[d2][0] = kq * 4096 + (4 * h2 + trq) * 128 + ((d2 ^ (trq >> 1)) << 6) + 32 * trb + 8 * trp;
    ob[d2][1] = ob[d2][0] + RING_HI;
    PIN(ob[d2][0]); PIN(ob[d2][1]);
  }
  unsigned fho = OFF_FH + (unsigned)(p * 4 + kq) * 128u + (lane & 3) * 32u;
  PIN(fho);
  unsigned da[4], c0, c1, oo, sb, xb;
  {
    const unsigned rb = 16384u * (2 * tp2 + (r32 >> 4)) + kq * 4096 + (r32 & 15) * 256;
#pragma unroll
    for (int g = 0; g < 4; ++g) { da[g] = rb + (((2 * g + h2) ^ (r32 & 7)) << 4); PIN(da[g]); }
    const unsigned t = tid >> 3, c8 = tid & 7, tr = t & 15;
    sb = L_FFT + (c8 >> 1) * 4096 + (t & 3) * 1024 + (c8 & 1) * 512 + (t >> 2) * 16;
    xb = L_XR + tid * 16;
    c0 = 16384u * (w >> 1) + tr * 256 + (((2 * c8) ^ (tr & 7)) << 4);
    c1 = c0 ^ 16u;
    oo = (128 * p + 64 * th + t) * 896 + dt * 128 + c8 * 16;
    PIN(c0); PIN(c1); PIN(oo); PIN(sb); PIN(xb);
  }
  f32x4 s0, s1;
  float r[8];
  wait_vm<0>();
  bar();
  STAMP(15);
  STAMPX(4, 11);
  f32x16 oa[2] = {splat16(0.f), splat16(0.f)};
  {
    v4u wo[14][2];
    auto rd_wo = [&](int i) __attribute__((always_inline)) {
#pragma unroll
      for (int d2 = 0; d2 < 2; ++d2) {
        const unsigned rb = ring_base(i >> 1);
        const unsigned a0 = (rb >= RING_HI ? ob[d2][1] + (rb - RING_HI) : ob[d2][0] + rb) + (i & 1) * 1024;
        wo[i][d2] = tr_frag(L, a0, a0 + 2048);
      }
    };
#define WOA 5
    unsigned fhv;
    ld4_sc1<0>(fhv, wsr, fho, 0);
#pragma unroll
    for (int i = 0; i < WOA; ++i) rd_wo(i);
    SCHED();
    wait_vm<0>();
    PIN(fhv);
    if (!wave_all(fhv == tag)) poll_flags<HSL>(wsr, fho, true, tag, 6u);
    STAMP(16);
    v4u hf[14];
    const unsigned so = OFF_H + (unsigned)((p * 4 + 2 * th + tp2) * 56 + 14 * kq) * 1024u;
#define HLAG 10
    static_for<0, 14 + HLAG>([&](auto ic) __attribute__((always_inline)) {
      constexpr int i = decltype(ic)::value, m = i - HLAG;
      if constexpr (i < 14) ld16_sc1<(i & 3) * 1024>(hf[i], wsr, lane * 16u, so + (i >> 2) * 4096);
      SCHED();
      if constexpr (WOA + i < 14) rd_wo(WOA + i);
      if constexpr (WOA + i == 13) grp_arrive(L, L_CTR + 40);
      SCHED();
      if constexpr (m >= 0) {
        asm volatile("s_waitcnt vmcnt(%1)" : "+v"(hf[m]) : "i"((i < 14 ? i : 13) - m) : "memory");
        oa[0] = mfma32(wo[m][0], hf[m], oa[0]);
        oa[1] = mfma32(wo[m][1], hf[m], oa[1]);
        SCHED();
        if constexpr (m == 11) grp_wait(L, L_CTR + 40, 8u);
      }
      SCHED();
    });
  }
  STAMP(17);
#pragma unroll
  for (int d2 = 0; d2 < 2; ++d2)
#pragma unroll
    for (int g = 0; g < 4; ++g) *(LAS f32x4*)(L + da[g] + d2 * 128) = f32x4{oa[d2][4 * g], oa[d2][4 * g + 1], oa[d2][4 * g + 2], oa[d2][4 * g + 3]};
  {
    s0 = *(LAS f32x4*)(L + sb); s1 = *(LAS f32x4*)(L + sb + 256);
    const v4u xq = *(LAS v4u*)(L + xb);
    bar();
    f32x4 o0[4], o1[4];
#pragma unroll
    for (int q = 0; q < 4; ++q) {
      o0[q] = *(LAS f32x4*)(L + q * 4096 + c0);
      o1[q] = *(LAS f32x4*)(L + q * 4096 + c1);
    }
    SCHED();
    float o[8], u[8];
#pragma unroll
    for (int e = 0; e < 4; ++e) {
      o[e] = (o0[0][e] + o0[1][e]) + (o0[2][e] + o0[3][e]);
      o[4 + e] = (o1[0][e] + o1[1][e]) + (o1[2][e] + o1[3][e]);
    }
#pragma unroll
    for (int e = 0; e < 8; ++e) u[e] = o[e] * -1.442695041f;
    SCHED();
#pragma unroll
    for (int e = 0; e < 8; ++e) u[e] = __builtin_amdgcn_exp2f(u[e]);
#pragma unroll
    for (int e = 0; e < 8; ++e) o[e] *= e < 4 ? s0[e & 3] : s1[e & 3];
    SCHED();
#pragma unroll
    for (int e = 0; e < 8; ++e) u[e] = __builtin_fmaf(u[e], 224.0f, 224.0f);
    SCHED();
#pragma unroll
    for (int e = 0; e < 8; ++e) u[e] = __builtin_amdgcn_rcpf(u[e]);
#pragma unroll
    for (int e = 0; e < 8; ++e) r[e] = (e & 1) ? bf_hi(xq[e >> 1]) : bf_lo(xq[e >> 1]);
    SCHED();
#pragma unroll
    for (int e = 0; e < 8; ++e) r[e] = __builtin_fmaf(o[e], u[e], r[e]);
    const v4u res = v4u{pk_bf(r[0], r[1]), pk_bf(r[2], r[3]), pk_bf(r[4], r[5]), pk_bf(r[6], r[7])};
    *(GAS v4u*)((GAS unsigned char*)P.out + oo) = res;
  }
  if (bid == 0 && tid == 0) st4_sc1<0>(tag, wsr, OFF_EPOCH, 0);
  STAMP(18);
  STAMP_OUT();
}
__device__ __attribute__((used, noinline)) void code_end_marker() { asm volatile("code_end_lbl:"); }

extern "C" void kernel_launch(void* const* d_in, const int* in_sizes, int n_in, void* d_out, int out_size, void* d_ws, size_t ws_size,
                              hipStream_t stream) {
  Params P;
  P.x = (const unsigned short*)d_in[0];
  P.wr = (const unsigned short*)d_in[1];
  P.wi = (const unsigned short*)d_in[2];
  P.win = (const unsigned short*)d_in[4];
  P.wsp = (const unsigned short*)d_in[5];
  P.bsp = (const unsigned short*)d_in[6];
  P.wout = (const unsigned short*)d_in[7];
  P.out = (unsigned short*)d_out;
  P.ws = (unsigned char*)d_ws;
  fused_kernel<<<256, 512, 0, stream>>>(P);
}
```
